```python
import torch

def _softmax(x, axis=-1):
    return torch.softmax(x, dim=axis)
def _gelu(x):
    return torch.nn.functional.gelu(x, approximate="tanh")
def _silu(x):
    return torch.nn.functional.silu(x)
def _sigmoid(x):
    return torch.sigmoid(x)
def _layernorm(x, axis=-1, eps=1e-5):
    m = x.mean(dim=axis, keepdim=True)
    v = x.var(dim=axis, keepdim=True, unbiased=False)
    return (x - m) / torch.sqrt(v + eps)
def _scatter_add(tgt, idx, src, axis=0):
    return tgt.clone().index_add_(axis, idx, src)
def _segment_sum(src, seg, n):
    out = torch.zeros((n,) + src.shape[1:], dtype=src.dtype)
    return out.index_add_(0, seg, src)
def _pad(x, pw):
    flat = tuple(v for pair in reversed(pw) for v in pair)
    return torch.nn.functional.pad(x, flat)

kernel_name = 'recurrent_scan__linear_attn_block__dense_activation_squared_relu__prompt'

def setup_inputs():

    _w = (lambda a: (lambda t: t.to(torch.bfloat16) if t.is_floating_point() else t)(torch.as_tensor(a)))
    def _A_setup():
        B, S, D = 1, 2048, 448
        return {
            "x": _w((lambda *s: torch.randn(*s, dtype=torch.bfloat16) * 0.05)(B, S, D)),
            "wqkv": _w((lambda *s: torch.randn(*s, dtype=torch.bfloat16) * 0.1)(D, 3 * D)),
            "wo": _w((lambda *s: torch.randn(*s, dtype=torch.bfloat16) * 0.05)(D, D)),
        }
    _A_in = _A_setup()

    def _B_setup():
        B, S, D, F = 1, 2048, 448, 1792
        return {
            "x": _w((lambda *s: torch.randn(*s, dtype=torch.bfloat16) * 0.05)(B, S, D)),
            "w1": _w((lambda *s: torch.randn(*s, dtype=torch.bfloat16) * 0.05)(D, F)),
            "b1": _w((lambda *s: torch.randn(*s, dtype=torch.bfloat16) * 0.2)(F)),
            "w2": _w((lambda *s: torch.randn(*s, dtype=torch.bfloat16) * 0.05)(F, D)),
            "b2": _w((lambda *s: torch.randn(*s, dtype=torch.bfloat16) * 0.8)(D)),
        }
    _B_in = _B_setup()

    return {
        "A_x": _A_in["x"],
        "A_wqkv": _A_in["wqkv"],
        "A_wo": _A_in["wo"],
        "B_x": _B_in["x"],
        "B_w1": _B_in["w1"],
        "B_b1": _B_in["b1"],
        "B_w2": _B_in["w2"],
        "B_b2": _B_in["b2"],
    }

def reference(A_x, A_wqkv, A_wo, B_x, B_w1, B_b1, B_w2, B_b2):
    def _A_ref(x, wqkv, wo):
        B, S, D = x.shape
        H = 16; Dh = D // H
        qkv = (lambda x, *d: x.permute(*d))((x @ wqkv).reshape(B, S, 3, H, Dh), 2, 0, 3, 1, 4)
        q, k, v = qkv[0], qkv[1], qkv[2]
        phi = lambda t: torch.where(t > 0, t + 1, torch.exp(t)) + 1e-6
        pq, pk = phi(q), phi(k)
        kv = torch.einsum("bhsd,bhse->bhde", pk, v)
        z = torch.einsum("bhsd->bhd", pk)
        num = torch.einsum("bhqd,bhde->bhqe", pq, kv)
        den = torch.einsum("bhqd,bhd->bhq", pq, z)[..., None] + 1e-6
        o = num / den
        return (lambda x, *d: x.permute(*d))(o, 0, 2, 1, 3).reshape(B, S, D) @ wo

    def _B_ref(x, w1, b1, w2, b2):
        h = torch.einsum("bsd,df->bsf", x, w1) + b1
        zero = torch.zeros_like(h)
        r = torch.where(h > zero, h, zero)
        h = r * r
        out = torch.einsum("bsf,fd->bsd", h, w2) + b2
        return out

    x = A_x
    xn = _layernorm(x, axis=-1)
    s = _A_ref(xn, A_wqkv, A_wo)
    g = _silu(_B_ref(xn, B_w1, B_b1, B_w2, B_b2))
    return x + s * g
```

```cpp
#include <hip/hip_runtime.h>
#include <hip/hip_bf16.h>
namespace naive {
typedef unsigned short u16;
__device__ __forceinline__ float b2f(u16 v){ return __uint_as_float(((unsigned)v)<<16); }
__device__ __forceinline__ u16 f2b(float f){ unsigned u=__float_as_uint(f); u += 0x7fffu + ((u>>16)&1u); return (u16)(u>>16); }
__device__ __forceinline__ float rb(float f){ return b2f(f2b(f)); }
constexpr int S=2048,D=448,F=1792,H=16,DH=28;
__global__ void k_ln(const u16* x, float* xn){
  int row=blockIdx.x; __shared__ float red[64];
  float s=0; for(int i=threadIdx.x;i<D;i+=64) s+=b2f(x[row*D+i]);
  red[threadIdx.x]=s; __syncthreads(); float t=0; for(int i=0;i<64;i++) t+=red[i]; __syncthreads();
  float m=t/D; float q=0; for(int i=threadIdx.x;i<D;i+=64){ float d=b2f(x[row*D+i])-m; q+=d*d; }
  red[threadIdx.x]=q; __syncthreads(); t=0; for(int i=0;i<64;i++) t+=red[i];
  float v=rb(t/D), mb=rb(m); float sd=rb(sqrtf(rb(v+1e-5f)));
  for(int i=threadIdx.x;i<D;i+=64) xn[row*D+i]=rb(rb(b2f(x[row*D+i])-mb)/sd);
}
__global__ void k_gemm(const float* A, const u16* W, float* C, int K, int N){
  int n=blockIdx.x*64+threadIdx.x, m=blockIdx.y; if(n>=N) return;
  float a=0; for(int k=0;k<K;k++) a+=A[m*K+k]*b2f(W[k*N+n]);
  C[m*N+n]=rb(a);
}
__device__ float phi(float t){ float r = t>0? rb(t+1.f): rb(expf(t)); return rb(r+1e-6f); }
__global__ void k_kv(const float* qkv, float* kv, float* z){
  int h=blockIdx.x, t=threadIdx.x; if(t>=DH*DH+DH) return;
  if(t<DH*DH){ int d=t/DH,e=t%DH; float a=0; for(int s=0;s<S;s++) a+=phi(qkv[s*3*D+D+h*DH+d])*qkv[s*3*D+2*D+h*DH+e]; kv[h*DH*DH+t]=rb(a);}
  else { int d=t-DH*DH; float a=0; for(int s=0;s<S;s++) a+=phi(qkv[s*3*D+D+h*DH+d]); z[h*DH+d]=rb(a);}
}
__global__ void k_o(const float* qkv,const float* kv,const float* z,float* o){
  int s=blockIdx.x,t=threadIdx.x,h=t/DH,e=t%DH; float num=0,den=0;
  for(int d=0;d<DH;d++){ float p=phi(qkv[s*3*D+h*DH+d]); num+=p*kv[h*DH*DH+d*DH+e]; den+=p*z[h*DH+d]; }
  o[s*D+t]=rb(rb(num)/rb(rb(den)+1e-6f));
}
__global__ void k_act(float* h,const u16* b1){ int i=blockIdx.x*256+threadIdx.x; float v=rb(h[i]+b2f(b1[i%F])); v=v>0?v:0; h[i]=rb(v*v);}
__global__ void k_fin(const u16* x,const float* s,const float* f,const u16* b2,u16* out){ int i=blockIdx.x*256+threadIdx.x; float v=rb(f[i]+b2f(b2[i%D])); float g=rb(v/(1.f+expf(-v))); out[i]=f2b(b2f(x[i])+rb(s[i]*g)); }
static void launch(void* const* d_in, void* d_out, void* d_ws, hipStream_t st){
  const u16* x=(const u16*)d_in[0]; const u16* wqkv=(const u16*)d_in[1]; const u16* wo=(const u16*)d_in[2];
  const u16* w1=(const u16*)d_in[4]; const u16* b1=(const u16*)d_in[5]; const u16* w2=(const u16*)d_in[6]; const u16* b2=(const u16*)d_in[7];
  float* xn=(float*)d_ws; float* qkv=xn+S*D; float* hh=qkv+S*3*D; float* o=hh+S*F; float* s=o+S*D; float* f=s+S*D; float* kv=f+S*D; float* z=kv+H*DH*DH;
  k_ln<<<S,64,0,st>>>(x,xn);
  k_gemm<<<dim3(21,S),64,0,st>>>(xn,wqkv,qkv,D,3*D);
  k_gemm<<<dim3(28,S),64,0,st>>>(xn,w1,hh,D,F);
  k_kv<<<H,1024,0,st>>>(qkv,kv,z);
  k_o<<<S,448,0,st>>>(qkv,kv,z,o);
  k_act<<<S*F/256,256,0,st>>>(hh,b1);
  k_gemm<<<dim3(7,S),64,0,st>>>(o,wo,s,D,D);
  k_gemm<<<dim3(7,S),64,0,st>>>(hh,w2,f,F,D);
  k_fin<<<S*D/256,256,0,st>>>(x,s,f,b2,(u16*)d_out);
}
}
extern "C" void kernel_launch(void* const* d_in, const int* in_sizes, int n_in, void* d_out, int out_size, void* d_ws, size_t ws_size, hipStream_t stream) {
  naive::launch(d_in,d_out,d_ws,stream);
}
```

```cpp
#include <hip/hip_runtime.h>
#include <stdint.h>
#include <type_traits>

#define LAS __attribute__((address_space(3)))
#define DI __device__ __forceinline__
typedef __attribute__((ext_vector_type(8))) __bf16 bf16x8;
typedef __attribute__((ext_vector_type(4))) __bf16 bf16x4;
typedef __attribute__((ext_vector_type(2))) __bf16 bf16x2;
typedef __attribute__((ext_vector_type(4))) float f32x4;
typedef __attribute__((ext_vector_type(2))) float f32x2;
typedef __attribute__((ext_vector_type(4))) unsigned u32x4;
typedef __attribute__((ext_vector_type(2))) unsigned u32x2;
typedef __amdgpu_buffer_rsrc_t rsrc_t;
typedef unsigned long long u64;

constexpr int S_ = 2048, D_ = 448, F_ = 1792;
constexpr int XROW = D_ * 2, QROW = 3 * D_ * 2, W1ROW = F_ * 2, W2ROW = D_ * 2;

constexpr unsigned WS_EPOCH = 0;
constexpr unsigned WS_FKV = 256;
constexpr unsigned WS_FH = 4096;
constexpr unsigned WS_FQ = 8192;
constexpr unsigned WS_FM = 12288;
constexpr unsigned WS_Z = 16384;
constexpr unsigned WS_M = 32768;
constexpr unsigned WS_PQ = WS_M + 14 * 28 * 1024 + 24576;
constexpr unsigned WS_K = WS_PQ + 128 * 14 * 1024;
constexpr unsigned WS_V = WS_K + 16 * 2 * 64 * 1024;
constexpr unsigned WS_H = WS_V + 16 * 2 * 64 * 1024;
constexpr unsigned WS_END = WS_H + 128 * 56 * 1024;
constexpr unsigned WS_BYTES = WS_END;

constexpr int NSLOT = 19;
constexpr unsigned L_SCR = NSLOT * 8192;
constexpr unsigned L_STAT = L_SCR;
constexpr unsigned L_CS = L_SCR + 1024;
constexpr unsigned L_ZF = L_SCR + 3072;
constexpr unsigned L_BYTES = 163840;
constexpr unsigned slot_of(int u) { return (unsigned)(u % NSLOT) * 8192u; }

constexpr unsigned SPIN_CAP = 1u << 17;

DI f32x4 mfma(bf16x8 a, bf16x8 b, f32x4 c) { return __builtin_amdgcn_mfma_f32_16x16x32_bf16(a, b, c, 0, 0, 0); }
DI rsrc_t mk_rsrc(const void* p, unsigned bytes) { return __builtin_amdgcn_make_buffer_rsrc((void*)p, 0, bytes, 0x00020000); }
DI void dma(rsrc_t rs, unsigned m0v, unsigned voff, unsigned soff) {
  asm volatile("s_mov_b32 m0, %0\n\ts_nop 0\n\tbuffer_load_dwordx4 %1, %2, %3 offen lds" ::"s"(m0v), "v"(voff), "s"(rs), "s"(soff) : "m0");
}
DI void ld128_sc1(u32x4& v, rsrc_t rs, unsigned voff, unsigned soff) {
  asm volatile("buffer_load_dwordx4 %0, %1, %2, %3 offen sc1" : "=v"(v) : "v"(voff), "s"(rs), "s"(soff));
}
DI void ld64(u32x2& v, rsrc_t rs, unsigned voff, unsigned soff) {
  asm volatile("buffer_load_dwordx2 %0, %1, %2, %3 offen" : "=v"(v) : "v"(voff), "s"(rs), "s"(soff));
}
DI void ld64_sc1(u32x2& v, rsrc_t rs, unsigned voff, unsigned soff) {
  asm volatile("buffer_load_dwordx2 %0, %1, %2, %3 offen sc1" : "=v"(v) : "v"(voff), "s"(rs), "s"(soff));
}
template <int N> DI void wait_bar() { asm volatile("s_waitcnt vmcnt(%0) lgkmcnt(0)\n\ts_barrier" ::"n"(N) : "memory"); }
DI void bar_only() { asm volatile("s_waitcnt lgkmcnt(0)\n\ts_barrier" ::: "memory"); }
template <int N, class T> DI void wait_tie(T& v) { asm volatile("s_waitcnt vmcnt(%1)" : "+v"(v) : "n"(N)); }
template <class T> DI void tie(T& v) { asm volatile("" : "+v"(v)); }
DI void st128_sc1(u32x4 v, rsrc_t rs, unsigned voff, unsigned soff) {
  asm volatile("buffer_store_dwordx4 %0, %1, %2, %3 offen sc1\n\ts_nop 1" ::"v"(v), "v"(voff), "s"(rs), "s"(soff) : "memory");
}
DI void st64_sc1(u32x2 v, rsrc_t rs, unsigned voff, unsigned soff) { __builtin_amdgcn_raw_buffer_store_b64(v, rs, voff, soff, 16); }
DI bf16x8 lds_frag(unsigned a) { return *(LAS const bf16x8*)(size_t)a; }
DI bf16x8 lds_tr(unsigned a0, unsigned a1) {
  bf16x4 lo = __builtin_amdgcn_ds_read_tr16_b64_v4bf16((LAS bf16x4*)(size_t)a0);
  bf16x4 hi = __builtin_amdgcn_ds_read_tr16_b64_v4bf16((LAS bf16x4*)(size_t)a1);
  return __builtin_shufflevector(lo, hi, 0, 1, 2, 3, 4, 5, 6, 7);
}
DI unsigned pk2(float a, float b) {
  f32x2 v = {a, b};
  return __builtin_bit_cast(unsigned, __builtin_convertvector(v, bf16x2));
}
DI float bf_lo(unsigned u) { return __builtin_bit_cast(float, u << 16); }
DI float bf_hi(unsigned u) { return __builtin_bit_cast(float, u & 0xffff0000u); }
DI float phi(float t) {
  float e = __builtin_amdgcn_exp2f(t * 1.44269504088896341f);
  return (t > 0.f ? t + 1.f : e) + 1e-6f;
}
DI bf16x8 ones_frag() {
  u32x4 o = {0x3f803f80u, 0x3f803f80u, 0x3f803f80u, 0x3f803f80u};
  return __builtin_bit_cast(bf16x8, o);
}

#define STAMP(i) do { } while (0)

struct Params {
  const char* x; const char* wqkv; const char* wo; const char* w1; const char* b1; const char* w2; const char* b2;
  char* out; char* ws;
};

DI void poll_flags(rsrc_t ws, unsigned voff, bool active, unsigned tlo, unsigned thi) {
#pragma unroll 1
  for (unsigned spins = 0; spins < SPIN_CAP; ++spins) {
    u32x2 v;
    asm volatile("buffer_load_dwordx2 %0, %1, %2, 0 offen sc1\n\ts_waitcnt vmcnt(0)" : "=&v"(v) : "v"(voff), "s"(ws) : "memory");
    bool ok = !active || (v.x == tlo && v.y == thi);
    if (__all(ok)) break;
    __builtin_amdgcn_s_sleep(2);
  }
}
DI void publish(rsrc_t ws, unsigned off, unsigned tlo, unsigned thi) {
  if (threadIdx.x == 0) { u32x2 t = {tlo, thi}; st64_sc1(t, ws, off, 0); }
}

struct Ctx {
  bf16x8 xf[2][14];
  unsigned ax[2][2];
  unsigned aw[2][2];
  f32x4 acc[2][2];
  f32x4 cs, ssum, ssq;
};

template <int ST, int UX, int UW, bool FIRST, bool TOKROW> DI void stage(Ctx& c) {
#pragma unroll
  for (int ks = 0; ks < 2; ++ks) {
    if (FIRST) {
      c.xf[0][2 * ST + ks] = lds_frag(slot_of(UX) + c.ax[0][ks]);
      c.xf[1][2 * ST + ks] = lds_frag(slot_of(UX) + c.ax[1][ks]);
    }
    bf16x8 w0 = lds_tr(slot_of(UW) + c.aw[ks][0], slot_of(UW) + c.aw[ks][0] + 512);
    bf16x8 w1 = lds_tr(slot_of(UW) + c.aw[ks][1], slot_of(UW) + c.aw[ks][1] + 512);
    const bf16x8 x0 = c.xf[0][2 * ST + ks], x1 = c.xf[1][2 * ST + ks];
    if (TOKROW) {
      c.acc[0][0] = mfma(x0, w0, c.acc[0][0]); c.acc[1][0] = mfma(x1, w0, c.acc[1][0]);
      c.acc[0][1] = mfma(x0, w1, c.acc[0][1]); c.acc[1][1] = mfma(x1, w1, c.acc[1][1]);
    } else {
      c.acc[0][0] = mfma(w0, x0, c.acc[0][0]); c.acc[1][0] = mfma(w0, x1, c.acc[1][0]);
      c.acc[0][1] = mfma(w1, x0, c.acc[0][1]); c.acc[1][1] = mfma(w1, x1, c.acc[1][1]);
    }
    if (ks == 0) c.cs = mfma(ones_frag(), w0, c.cs);
    if (FIRST) { c.ssum = mfma(x0, ones_frag(), c.ssum); c.ssq = mfma(x0, x0, c.ssq); }
  }
}
DI void zero_acc(Ctx& c) {
  f32x4 z = {0.f, 0.f, 0.f, 0.f};
  c.acc[0][0] = z; c.acc[0][1] = z; c.acc[1][0] = z; c.acc[1][1] = z; c.cs = z;
}

template <bool HELPER> DI void run(const Params& P, int c, int role) {
  const int tid = threadIdx.x, lane = tid & 63;
  const int w = __builtin_amdgcn_readfirstlane(tid >> 6);
  const int p = w & 3, hf = w >> 2, par = p >> 1, p1 = p & 1;
  const int l15 = lane & 15, g = lane >> 4;
  const rsrc_t rx = mk_rsrc(P.x, S_ * XROW), rq = mk_rsrc(P.wqkv, D_ * QROW), rws = mk_rsrc(P.ws, WS_BYTES);
  const rsrc_t rB = HELPER ? rq : mk_rsrc(P.w1, D_ * W1ROW);
  const rsrc_t rC = HELPER ? mk_rsrc(P.wo, D_ * W2ROW) : mk_rsrc(P.w2, F_ * W2ROW);
  constexpr unsigned BROW = HELPER ? QROW : W1ROW;
  const unsigned m0w = (unsigned)w * 1024u;
  STAMP(0);

  u32x2 ep;
  ld64_sc1(ep, rws, 0, WS_EPOCH);
  u32x2 bias[2][2];
  if (!HELPER) {
    const rsrc_t rb1 = mk_rsrc(P.b1, F_ * 2);
#pragma unroll
    for (int ps = 0; ps < 2; ++ps)
#pragma unroll
      for (int nt = 0; nt < 2; ++nt) ld64(bias[ps][nt], rb1, (unsigned)((16 * (2 * hf + (nt ^ p1)) + 4 * g) * 2), (unsigned)((128 * role + 64 * ps) * 2));
  }

  const int r8 = lane >> 3, ch = lane & 7;
  const int drow = 8 * w + r8;
  const unsigned vx = (unsigned)((c * 128 + drow) * XROW + ((ch ^ ((drow >> 1) & 7)) << 4));
  const int swzA = ((drow >> 1) & 1) | (((drow >> 3) & 1) << 1);
  const unsigned vq = (unsigned)(drow * QROW + ((ch ^ (swzA << 1)) << 4));
  const unsigned vB = (unsigned)(drow * BROW + ((ch ^ (swzA << 1)) << 4));
  const unsigned vC = (unsigned)(drow * W2ROW + ((ch ^ (((drow >> 1) & 3) << 1)) << 4));
  const int hl0 = HELPER ? (role == 14 ? 0 : 4) : 0;
  const unsigned col0 = HELPER ? (unsigned)(hl0 * 128) : (unsigned)(896 + role * 128);
  const unsigned col1 = HELPER ? col0 + 128u : (unsigned)(role * 256);
  const int th = role / 7, ct = role % 7;

  auto issue = [&](auto uc) {
    constexpr int u = decltype(uc)::value;
    if constexpr (u < 21) {
      constexpr int s = u / 3, k = u % 3;
      if constexpr (k == 0) dma(rq, slot_of(u) + m0w, vq, col0 + (unsigned)(s * 64 * QROW));
      else dma(rx, slot_of(u) + m0w, vx, (unsigned)((k - 1) * 64 * XROW + s * 128));
    } else if constexpr (u < 35 || (HELPER && u < 42)) {
      constexpr int ps = (u - 21) / 7, s = (u - 21) % 7;
      dma(rB, slot_of(u) + m0w, vB, col1 + (unsigned)(ps * 128 + s * 64 * BROW));
    } else if constexpr (!HELPER) {
      constexpr int v = u - 35;
      constexpr int row0 = v < 14 ? 128 * v : 128 * (v - 14) + 64;
      dma(rC, slot_of(u) + m0w, vC, (unsigned)(ct * 128 + row0 * W2ROW));
    }
  };
#define IC(n) std::integral_constant<int, (n)>{}
#define ISSUE3(n) do { issue(IC(n)); issue(IC((n) + 1)); issue(IC((n) + 2)); } while (0)

  ISSUE3(0); ISSUE3(3); ISSUE3(6);

  Ctx cx;
#pragma unroll
  for (int tt = 0; tt < 2; ++tt)
#pragma unroll
    for (int ks = 0; ks < 2; ++ks) {
      const int T = 2 * p + (tt ^ hf);
      cx.ax[tt][ks] = (unsigned)((T * 16 + l15) * 128 + (((4 * (ks ^ par) + g) ^ ((l15 >> 1) & 7)) << 4));
    }
  {
    const int q = l15 >> 2, pp = lane & 3;
    const int swz = ((q >> 1) & 1) | ((g & 1) << 1);
#pragma unroll
    for (int ks = 0; ks < 2; ++ks)
#pragma unroll
      for (int nt = 0; nt < 2; ++nt)
        cx.aw[ks][nt] = (unsigned)((32 * (ks ^ par) + 8 * g + q) * 128 + (((2 * hf + (nt ^ p1)) ^ swz) << 5) + (pp << 3));
  }
  zero_acc(cx);
  cx.ssum = cx.cs; cx.ssq = cx.cs;

#define P0STAGE(s) do { wait_bar<6>(); ISSUE3(9 + 3 * (s)); stage<s, 3 * (s) + 1, 3 * (s), true, !HELPER>(cx); } while (0)
  P0STAGE(0);
  tie(ep);
  if (!HELPER) { tie(bias[0][0]); tie(bias[0][1]); tie(bias[1][0]); tie(bias[1][1]); }
  STAMP(1);
  P0STAGE(1); P0STAGE(2); P0STAGE(3); P0STAGE(4); P0STAGE(5); P0STAGE(6);
  unsigned tlo, thi;
  {
    const u64 e = ((u64)(unsigned)__builtin_amdgcn_readfirstlane(ep.y) << 32) | (unsigned)__builtin_amdgcn_readfirstlane(ep.x);
    const u64 t = e * 0x9E3779B97F4A7C15ull + 0x632BE59BD9B4E019ull;
    tlo = (unsigned)t; thi = (unsigned)(t >> 32);
  }
  auto put_cs = [&](int pass) {
    if (lane < 16) *(LAS float*)(size_t)(L_CS + pass * 512 + par * 256 + (16 * (2 * hf + p1) + lane) * 4) = cx.cs[0];
  };
  {
    const int r = lane & 3;
    const float sm = r == 0 ? cx.ssum[0] : r == 1 ? cx.ssum[1] : r == 2 ? cx.ssum[2] : cx.ssum[3];
    const float sq = r == 0 ? cx.ssq[0] : r == 1 ? cx.ssq[1] : r == 2 ? cx.ssq[2] : cx.ssq[3];
    const float mean = sm * (1.f / D_);
    const float var = fmaxf(sq * (1.f / D_) - mean * mean, 0.f);
    const float rs = __builtin_amdgcn_rsqf(var + 1e-5f);
    f32x2 st = {rs, -mean * rs};
    if ((l15 >> 2) == g) *(LAS f32x2*)(size_t)(L_STAT + ((2 * p + hf) * 16 + l15) * 8) = st;
  }
  put_cs(0);
  wait_bar<5>();
  STAMP(2);
  f32x2 stc[2];
#pragma unroll
  for (int tt = 0; tt < 2; ++tt) stc[tt] = *(LAS const f32x2*)(size_t)(L_STAT + ((2 * p + (tt ^ hf)) * 16 + l15) * 8);

  auto epi_col = [&](int pass, unsigned dst_base, unsigned tile_stride) {
    f32x4 cv[2];
#pragma unroll
    for (int nt = 0; nt < 2; ++nt) {
      const unsigned a = L_CS + pass * 512 + (16 * (2 * hf + (nt ^ p1)) + 4 * g) * 4;
      cv[nt] = *(LAS const f32x4*)(size_t)a + *(LAS const f32x4*)(size_t)(a + 256);
    }
#pragma unroll
    for (int tt = 0; tt < 2; ++tt) {
      unsigned o[2][2];
#pragma unroll
      for (int nt = 0; nt < 2; ++nt) {
        float v[4];
#pragma unroll
        for (int r = 0; r < 4; ++r) {
          float t = stc[tt].y * cv[nt][r];
          if constexpr (!HELPER) { const unsigned bu = bias[pass - 1][nt][r >> 1]; t += (r & 1) ? bf_hi(bu) : bf_lo(bu); }
          t = fmaf(stc[tt].x, cx.acc[tt][nt][r], t);
          if (HELPER) v[r] = phi(t);
          else { t = fmaxf(t, 0.f); v[r] = t * t; }
        }
        o[nt][0] = pk2(v[0], v[1]); o[nt][1] = pk2(v[2], v[3]);
      }
      u32x4 d;
      if (p1) d = u32x4{o[1][0], o[1][1], o[0][0], o[0][1]}; else d = u32x4{o[0][0], o[0][1], o[1][0], o[1][1]};
      st128_sc1(d, rws, (unsigned)(lane * 16), dst_base + (unsigned)(8 * c + 2 * p + (tt ^ hf)) * tile_stride + (unsigned)hf * 1024u);
    }
  };

  if (HELPER) {
    epi_col(0, WS_PQ + (unsigned)(2 * hl0) * 1024u, 14 * 1024);
  } else {
    const bool isk = role < 7;
    f32x4 sr[2][2];
#pragma unroll
    for (int tt = 0; tt < 2; ++tt) {
      const unsigned a = L_STAT + ((2 * p + (tt ^ hf)) * 16 + 4 * g) * 8;
      sr[tt][0] = *(LAS const f32x4*)(size_t)a; sr[tt][1] = *(LAS const f32x4*)(size_t)(a + 16);
    }
#pragma unroll
    for (int nt = 0; nt < 2; ++nt) {
      const int nl = 16 * (2 * hf + (nt ^ p1)) + l15;
      const float cv = *(LAS const float*)(size_t)(L_CS + nl * 4) + *(LAS const float*)(size_t)(L_CS + 256 + nl * 4);
      unsigned o[2][2];
#pragma unroll
      for (int tt = 0; tt < 2; ++tt) {
        float v[4];
#pragma unroll
        for (int r = 0; r < 4; ++r) {
          const float t = fmaf(sr[tt][r >> 1][2 * (r & 1)], cx.acc[tt][nt][r], sr[tt][r >> 1][2 * (r & 1) + 1] * cv);
          v[r] = isk ? phi(t) : t;
        }
        o[tt][0] = pk2(v[0], v[1]); o[tt][1] = pk2(v[2], v[3]);
      }
      u32x4 d;
      if (hf) d = u32x4{o[1][0], o[1][1], o[0][0], o[0][1]}; else d = u32x4{o[0][0], o[0][1], o[1][0], o[1][1]};
      const int n = (isk ? role : role - 7) * 64 + nl, hd = n / 28, dd = n - 28 * hd;
      const unsigned voff = (unsigned)(((hd * 2 + (dd >> 4)) * 64) * 1024 + (g * 16 + (dd & 15)) * 16);
      st128_sc1(d, rws, voff, (isk ? WS_K : WS_V) + (unsigned)(4 * c + p) * 1024u);
    }
  }
  ISSUE3(30); ISSUE3(33);
  zero_acc(cx);
  stage<0, 0, 21, false, false>(cx); stage<1, 0, 22, false, false>(cx); stage<2, 0, 23, false, false>(cx); stage<3, 0, 24, false, false>(cx);
  wait_bar<6>();
  if (!HELPER) publish(rws, WS_FKV + (unsigned)(role * 16 + c) * 8u, tlo, thi);
  STAMP(3);
  ISSUE3(36); ISSUE3(39);
  stage<4, 0, 25, false, false>(cx); stage<5, 0, 26, false, false>(cx); stage<6, 0, 27, false, false>(cx);
  put_cs(1);
  wait_bar<10>();
  STAMP(4);
  if (HELPER) epi_col(1, WS_PQ + (unsigned)(2 * hl0 + 2) * 1024u, 14 * 1024);
  else epi_col(1, WS_H + (unsigned)(4 * role) * 1024u, 56 * 1024);

  if constexpr (!HELPER) {
    ISSUE3(42); issue(IC(45)); issue(IC(46));
    zero_acc(cx);
    stage<0, 0, 28, false, false>(cx); stage<1, 0, 29, false, false>(cx); stage<2, 0, 30, false, false>(cx); stage<3, 0, 31, false, false>(cx);
    wait_bar<5>();
    publish(rws, WS_FH + (unsigned)(c * 32 + role) * 8u, tlo, thi);
    STAMP(5);
    ISSUE3(47); issue(IC(50));
    stage<4, 0, 32, false, false>(cx); stage<5, 0, 33, false, false>(cx); stage<6, 0, 34, false, false>(cx);
    put_cs(2);
    bar_only();
    STAMP(6);
    epi_col(2, WS_H + (unsigned)(4 * role + 2) * 1024u, 56 * 1024);
    ISSUE3(51);
    if (w == 0) poll_flags(rws, WS_FH + (unsigned)(c * 32 + l15) * 8u, lane < 14, tlo, thi);
    wait_bar<0>();
    publish(rws, WS_FH + (unsigned)(c * 32 + 16 + role) * 8u, tlo, thi);
    STAMP(7);

    const int tt = w & 3, kh = w >> 2;
    const unsigned tokt = (unsigned)(8 * c + 4 * th + tt);
    u32x4 hfr[28];
    const unsigned hbase = WS_H + tokt * 56u * 1024u + (unsigned)kh * 1024u;
    const unsigned vl = (unsigned)lane * 16u;
#pragma unroll
    for (int v = 0; v < 14; ++v) ld128_sc1(hfr[v], rws, vl, hbase + (unsigned)(4 * v) * 1024u);
    u32x2 rb2[2], rxr[2];
    const unsigned ocol = (unsigned)((ct * 64 + 32 * kh + 4 * g) * 2);
    const unsigned orow = (tokt * 16u + (unsigned)l15) * XROW;
    {
      const rsrc_t rbb = mk_rsrc(P.b2, D_ * 2);
      ld64(rb2[0], rbb, ocol, 0); ld64(rb2[1], rbb, ocol, 32);
      ld64(rxr[0], rx, orow + ocol, 0); ld64(rxr[1], rx, orow + ocol, 32);
    }
    unsigned bw[4];
    {
      const int q = l15 >> 2, pp = lane & 3, row = 32 * kh + 4 * g + q;
#pragma unroll
      for (int t = 0; t < 4; ++t) bw[t] = (unsigned)(row * 128 + ((t ^ ((row >> 1) & 3)) << 5) + (pp << 3));
    }
    f32x4 fa[4], sa[4];
#pragma unroll
    for (int t = 0; t < 4; ++t) { fa[t] = f32x4{0.f, 0.f, 0.f, 0.f}; sa[t] = fa[t]; }
    auto unitB = [&](auto vc, auto nc) {
      constexpr int v = decltype(vc)::value, n = decltype(nc)::value;
      bf16x8 wf[4];
#pragma unroll
      for (int t = 0; t < 4; ++t) wf[t] = lds_tr(slot_of(35 + v) + bw[t], slot_of(35 + v) + bw[t] + 2048);
      wait_tie<n>(hfr[v]);
#pragma unroll
      for (int t = 0; t < 4; ++t) fa[t] = mfma(wf[t], __builtin_bit_cast(bf16x8, hfr[v]), fa[t]);
    };
    unitB(IC(0), IC(17)); unitB(IC(1), IC(16)); unitB(IC(2), IC(15)); unitB(IC(3), IC(14));
    unitB(IC(4), IC(13)); unitB(IC(5), IC(12)); unitB(IC(6), IC(11));
    if (w == 0) poll_flags(rws, WS_FH + (unsigned)(c * 32 + 16 + l15) * 8u, lane < 14, tlo, thi);
    bar_only();
    STAMP(8);
#pragma unroll
    for (int v = 14; v < 19; ++v) ld128_sc1(hfr[v], rws, vl, hbase + (unsigned)(4 * (v - 14) + 2) * 1024u);
    ISSUE3(54); ISSUE3(57); issue(IC(60));
#pragma unroll
    for (int v = 19; v < 28; ++v) ld128_sc1(hfr[v], rws, vl, hbase + (unsigned)(4 * (v - 14) + 2) * 1024u);
    unitB(IC(7), IC(31)); unitB(IC(8), IC(30)); unitB(IC(9), IC(29)); unitB(IC(10), IC(28));
    unitB(IC(11), IC(27)); unitB(IC(12), IC(26)); unitB(IC(13), IC(25));
    bar_only();
    issue(IC(61)); issue(IC(62));
    unitB(IC(14), IC(22)); unitB(IC(15), IC(21)); unitB(IC(16), IC(20)); unitB(IC(17), IC(19)); unitB(IC(18), IC(18));
    tie(rb2[0]); tie(rb2[1]); tie(rxr[0]); tie(rxr[1]);
    wait_bar<11>();
    STAMP(9);
    unitB(IC(19), IC(10)); unitB(IC(20), IC(9)); unitB(IC(21), IC(8)); unitB(IC(22), IC(7));
    unitB(IC(23), IC(6)); unitB(IC(24), IC(5)); unitB(IC(25), IC(4));
    if (w == 0) poll_flags(rws, lane < 32 ? WS_FM + (unsigned)lane * 8u : WS_FQ + (unsigned)(c * 16 + (lane & 1)) * 8u, lane < 34, tlo, thi);
    wait_bar<0>();
    STAMP(10);
    if (blockIdx.x == 0) publish(rws, WS_EPOCH, tlo, thi);
    u32x4 mreg[7], pq[7], zreg;
#pragma unroll
    for (int i = 0; i < 7; ++i) {
      const int f = w * 7 + i;
      ld128_sc1(mreg[i], rws, vl, WS_M + (unsigned)(((f >> 2) * 28 + 4 * ct + (f & 3)) * 1024));
    }
    ld128_sc1(zreg, rws, vl, WS_Z);
#pragma unroll
    for (int i = 0; i < 7; ++i) ld128_sc1(pq[i], rws, vl, WS_PQ + (tokt * 14u + (unsigned)(7 * kh + i)) * 1024u);
    unitB(IC(26), IC(15)); unitB(IC(27), IC(15));
    constexpr unsigned L_M = slot_of(44);
    static_assert(slot_of(50) == L_M + 6 * 8192, "M staging must be contiguous");
    wait_tie<7>(zreg);
#pragma unroll
    for (int i = 0; i < 7; ++i) { tie(mreg[i]); *(LAS u32x4*)(size_t)(L_M + (unsigned)(w * 7 + i) * 1024u + vl) = mreg[i]; }
    if (w == 0 && lane < 56) *(LAS u32x4*)(size_t)(L_ZF + vl) = zreg;
    wait_bar<0>();
    STAMP(11);
#pragma unroll
    for (int i = 0; i < 7; ++i) tie(pq[i]);
    f32x4 dA = {0.f, 0.f, 0.f, 0.f}, dB = dA;
#pragma unroll
    for (int i = 0; i < 7; ++i) {
      const u32x4 zf = *(LAS const u32x4*)(size_t)(L_ZF + (unsigned)((7 * kh + i) * 64 + g * 16));
      const int h0 = (8 * i + g) / 7, h1 = (8 * i + 4 + g) / 7, r = lane & 3;
      if (i <= 3) {
        u32x4 a = {h0 == r ? zf.x : 0u, h0 == r ? zf.y : 0u, h1 == r ? zf.z : 0u, h1 == r ? zf.w : 0u};
        dA = mfma(__builtin_bit_cast(bf16x8, a), __builtin_bit_cast(bf16x8, pq[i]), dA);
      }
      if (i >= 3) {
        u32x4 a = {h0 == r + 4 ? zf.x : 0u, h0 == r + 4 ? zf.y : 0u, h1 == r + 4 ? zf.z : 0u, h1 == r + 4 ? zf.w : 0u};
        dB = mfma(__builtin_bit_cast(bf16x8, a), __builtin_bit_cast(bf16x8, pq[i]), dB);
      }
    }
    float inv[8];
#pragma unroll
    for (int r = 0; r < 4; ++r) { inv[r] = __builtin_amdgcn_rcpf(dA[r] + 1e-6f); inv[4 + r] = __builtin_amdgcn_rcpf(dB[r] + 1e-6f); }
#pragma unroll
    for (int i = 0; i < 7; ++i) {
      u32x4 sc;
#pragma unroll
      for (int hh = 0; hh < 2; ++hh) {
        const int u4 = 8 * i + 4 * hh, lo = u4 / 7, hi = (u4 + 3) / 7;
        const float f = (lo == hi) ? inv[lo] : ((u4 + g) / 7 == lo ? inv[lo] : inv[hi]);
        sc[2 * hh] = pk2(bf_lo(pq[i][2 * hh]) * f, bf_hi(pq[i][2 * hh]) * f);
        sc[2 * hh + 1] = pk2(bf_lo(pq[i][2 * hh + 1]) * f, bf_hi(pq[i][2 * hh + 1]) * f);
      }
#pragma unroll
      for (int t = 0; t < 4; ++t)
        sa[t] = mfma(lds_frag(L_M + (unsigned)(((7 * kh + i) * 4 + t) * 1024) + vl), __builtin_bit_cast(bf16x8, sc), sa[t]);
    }
    constexpr unsigned L_X = slot_of(51);
    static_assert(slot_of(54) == L_X + 3 * 8192, "exchange area must be contiguous");
    {
      const unsigned a = L_X + (unsigned)w * 4096u + vl;
      *(LAS f32x4*)(size_t)(a) = kh ? fa[0] : fa[2];
      *(LAS f32x4*)(size_t)(a + 1024) = kh ? fa[1] : fa[3];
      *(LAS f32x4*)(size_t)(a + 2048) = kh ? sa[0] : sa[2];
      *(LAS f32x4*)(size_t)(a + 3072) = kh ? sa[1] : sa[3];
    }
    bar_only();
    {
      const unsigned a = L_X + (unsigned)(w ^ 4) * 4096u + vl;
      const rsrc_t ro = mk_rsrc(P.out, S_ * XROW);
#pragma unroll
      for (int t = 0; t < 2; ++t) {
        const f32x4 f = (kh ? fa[2 + t] : fa[t]) + *(LAS const f32x4*)(size_t)(a + t * 1024);
        const f32x4 s = (kh ? sa[2 + t] : sa[t]) + *(LAS const f32x4*)(size_t)(a + 2048 + t * 1024);
        float o[4];
#pragma unroll
        for (int r = 0; r < 4; ++r) {
          const float y = f[r] + ((r & 1) ? bf_hi(rb2[t][r >> 1]) : bf_lo(rb2[t][r >> 1]));
          const float sg = y * __builtin_amdgcn_rcpf(1.f + __builtin_amdgcn_exp2f(-1.44269504088896341f * y));
          o[r] = fmaf(s[r], sg, (r & 1) ? bf_hi(rxr[t][r >> 1]) : bf_lo(rxr[t][r >> 1]));
        }
        u32x2 d = {pk2(o[0], o[1]), pk2(o[2], o[3])};
        __builtin_amdgcn_raw_buffer_store_b64(d, ro, orow + ocol, (unsigned)(t * 32), 0);
      }
    }
    STAMP(12);
  } else {
    const int hidx = 2 * c + (role - 14), hd = hidx >> 1, dt = hidx & 1;
    constexpr unsigned L_WO = slot_of(42);
    static_assert(slot_of(45) == L_WO + 3 * 8192, "wo staging must be contiguous");
    if (w < 7) {
      const unsigned vo = (unsigned)(r8 * W2ROW + ((ch ^ (((r8 >> 1) & 3) << 1)) << 4));
#pragma unroll
      for (int o = 0; o < 4; ++o) dma(rC, L_WO + (unsigned)w * 4096u + o * 1024u, vo, (unsigned)((28 * hd + 8 * o) * W2ROW + w * 128));
    }
    zero_acc(cx);
    stage<0, 0, 28, false, false>(cx); stage<1, 0, 29, false, false>(cx); stage<2, 0, 30, false, false>(cx); stage<3, 0, 31, false, false>(cx);
    wait_bar<2>();
    stage<4, 0, 32, false, false>(cx); stage<5, 0, 33, false, false>(cx); stage<6, 0, 34, false, false>(cx);
    put_cs(2);
    bar_only();
    epi_col(2, WS_PQ + (unsigned)(2 * hl0 + 4) * 1024u, 14 * 1024);
    if (role == 14) {
      zero_acc(cx);
      stage<0, 0, 35, false, false>(cx); stage<1, 0, 36, false, false>(cx); stage<2, 0, 37, false, false>(cx); stage<3, 0, 38, false, false>(cx);
      stage<4, 0, 39, false, false>(cx); stage<5, 0, 40, false, false>(cx); stage<6, 0, 41, false, false>(cx);
      put_cs(3);
      bar_only();
      epi_col(3, WS_PQ + 6u * 1024u, 14 * 1024);
    }
    STAMP(5);
    if (w == 0) {
      const int lo = (28 * hd) >> 6, hi = (28 * hd + 27) >> 6;
      const int line = ((g & 1) ? hi : lo) + ((g >> 1) ? 7 : 0);
      poll_flags(rws, WS_FKV + (unsigned)(line * 16 + l15) * 8u, true, tlo, thi);
    }
    wait_bar<0>();
    publish(rws, WS_FQ + (unsigned)(c * 16 + (role - 14)) * 8u, tlo, thi);
    STAMP(6);
    const unsigned vl = (unsigned)lane * 16u;
    u32x4 kf[8], v0[8], v1[8];
#pragma unroll
    for (int i = 0; i < 8; ++i) {
      const unsigned ts = (unsigned)(8 * w + i);
      ld128_sc1(kf[i], rws, vl, WS_K + ((unsigned)(hd * 2 + dt) * 64u + ts) * 1024u);
      ld128_sc1(v0[i], rws, vl, WS_V + ((unsigned)(hd * 2) * 64u + ts) * 1024u);
      ld128_sc1(v1[i], rws, vl, WS_V + ((unsigned)(hd * 2 + 1) * 64u + ts) * 1024u);
    }
    f32x4 a0 = {0.f, 0.f, 0.f, 0.f}, a1 = a0;
    const bool kpad = dt == 1 && l15 >= 12, vpad = l15 >= 12;
    const unsigned vfill = l15 == 12 ? 0x3f803f80u : 0u;
    auto kvstep = [&](auto ic) {
      constexpr int i = decltype(ic)::value;
      wait_tie<21 - 3 * i>(v1[i]); tie(kf[i]); tie(v0[i]);
      u32x4 k = kf[i], b = v1[i];
#pragma unroll
      for (int e = 0; e < 4; ++e) { k[e] = kpad ? 0u : k[e]; b[e] = vpad ? vfill : b[e]; }
      a0 = mfma(__builtin_bit_cast(bf16x8, v0[i]), __builtin_bit_cast(bf16x8, k), a0);
      a1 = mfma(__builtin_bit_cast(bf16x8, b), __builtin_bit_cast(bf16x8, k), a1);
    };
    kvstep(IC(0)); kvstep(IC(1)); kvstep(IC(2)); kvstep(IC(3)); kvstep(IC(4)); kvstep(IC(5)); kvstep(IC(6)); kvstep(IC(7));
    constexpr unsigned L_KV = 10 * 8192;
    *(LAS f32x4*)(size_t)(L_KV + (unsigned)w * 2048u + vl) = a0;
    *(LAS f32x4*)(size_t)(L_KV + (unsigned)w * 2048u + 1024u + vl) = a1;
    bar_only();
    STAMP(7);
    a0 = f32x4{0.f, 0.f, 0.f, 0.f}; a1 = a0;
#pragma unroll
    for (int i = 0; i < 8; ++i) {
      a0 += *(LAS const f32x4*)(size_t)(L_KV + i * 2048u + vl);
      a1 += *(LAS const f32x4*)(size_t)(L_KV + i * 2048u + 1024u + vl);
    }
    u32x4 kvf = {pk2(a0[0], a0[1]), pk2(a0[2], a0[3]), pk2(a1[0], a1[1]), pk2(a1[2], a1[3])};
    if (w == 0 && g == 3 && 16 * dt + l15 < 28) {
      const int d = 28 * hd + 16 * dt + l15;
      const unsigned off = (unsigned)((d >> 5) * 64 + ((d >> 2) & 3) * 16 + ((d >> 4) & 1) * 8 + (d & 3) * 2);
      __builtin_amdgcn_raw_buffer_store_b16((unsigned short)(kvf.z & 0xffffu), rws, off, WS_Z, 16);
    }
    if (g == 3) { kvf.z = 0u; kvf.w = 0u; }
    {
      const int q = l15 >> 2, pp = lane & 3, row = 4 * g + q;
      const int d = 28 * hd + 16 * dt + 4 * g;
      const bool ok = 16 * dt + 4 * g < 28;
      const unsigned moff = (unsigned)((d >> 5) * 28 * 1024 + (((d >> 2) & 3) * 16 + l15) * 16 + ((d >> 4) & 1) * 8);
#pragma unroll
      for (int i = 0; i < 4; ++i) {
        const int nt = w + 8 * i;
        if (nt < 28) {
          const unsigned a = L_WO + (unsigned)((nt >> 2) * 4096 + row * 128 + (((nt & 3) ^ ((row >> 1) & 3)) << 5) + (pp << 3));
          const bf16x8 wf = lds_tr(a, a + 2048);
          const f32x4 m = mfma(__builtin_bit_cast(bf16x8, kvf), wf, f32x4{0.f, 0.f, 0.f, 0.f});
          u32x2 dta = {pk2(m[0], m[1]), pk2(m[2], m[3])};
          if (ok) st64_sc1(dta, rws, moff, WS_M + (unsigned)nt * 1024u);
        }
      }
    }
    wait_bar<0>();
    publish(rws, WS_FM + (unsigned)hidx * 8u, tlo, thi);
    STAMP(8);
  }
}

__global__ __launch_bounds__(512, 2) void fused_block_kernel(Params P) {
  __shared__ __attribute__((aligned(16))) char smem[L_BYTES];
  asm volatile("" ::"v"((unsigned)(size_t)(LAS char*)smem));
  const int b = blockIdx.x, xg = b & 7, r = b >> 3;
  const int c = 2 * xg + (r >> 4), role = r & 15;
  if (role < 14) run<false>(P, c, role);
  else run<true>(P, c, role);
}

extern "C" void kernel_launch(void* const* d_in, const int* in_sizes, int n_in, void* d_out, int out_size, void* d_ws, size_t ws_size,
                              hipStream_t stream) {
  Params P;
  P.x = (const char*)d_in[0]; P.wqkv = (const char*)d_in[1]; P.wo = (const char*)d_in[2];
  P.w1 = (const char*)d_in[4]; P.b1 = (const char*)d_in[5]; P.w2 = (const char*)d_in[6]; P.b2 = (const char*)d_in[7];
  P.out = (char*)d_out; P.ws = (char*)d_ws;
  fused_block_kernel<<<256, 512, 0, stream>>>(P);
}
```

```cpp
#include <hip/hip_runtime.h>
#include <stdint.h>
#include <type_traits>

#define LAS __attribute__((address_space(3)))
#define DI __device__ __forceinline__
#define AI __attribute__((always_inline))
typedef __attribute__((ext_vector_type(8))) __bf16 bf16x8;
typedef __attribute__((ext_vector_type(4))) __bf16 bf16x4;
typedef __attribute__((ext_vector_type(2))) __bf16 bf16x2;
typedef __attribute__((ext_vector_type(4))) float f32x4;
typedef __attribute__((ext_vector_type(2))) float f32x2;
typedef __attribute__((ext_vector_type(4))) unsigned u32x4;
typedef __attribute__((ext_vector_type(2))) unsigned u32x2;
typedef __amdgpu_buffer_rsrc_t rsrc_t;
typedef unsigned long long u64;

constexpr int S_ = 2048, D_ = 448, F_ = 1792;
constexpr int XROW = D_ * 2, QROW = 3 * D_ * 2, W1ROW = F_ * 2, W2ROW = D_ * 2;

constexpr unsigned WS_EPOCH = 0;
constexpr unsigned WS_FKV = 256;
constexpr unsigned WS_FM = 4096;
constexpr unsigned WS_FH = 8192;
constexpr unsigned WS_Z = 16384;
constexpr unsigned WS_ST = 32768;
constexpr unsigned WS_CS = 49152;
constexpr unsigned WS_M = 81920;
constexpr unsigned WS_PQ = WS_M + 16 * 28 * 1024;
constexpr unsigned KVG = 7424, KVT = 4 * KVG;
constexpr unsigned WS_K = WS_PQ + 128 * 14 * 1024;
constexpr unsigned WS_V = WS_K + 64 * KVT;
constexpr unsigned WS_H = WS_V + 64 * KVT;
constexpr unsigned WS_P = WS_H + 128 * 56 * 1024;
constexpr unsigned WS_END = WS_P + 224 * 16384;
constexpr unsigned WS_BYTES = WS_END;
constexpr unsigned OOB = 0x80000000u;

constexpr unsigned L_SCR = 19 * 8192;
constexpr unsigned L_STAT = L_SCR;
constexpr unsigned L_KVF = L_SCR + 1024;
constexpr unsigned L_BYTES = 163840;

constexpr unsigned CODE_BYTES = 22 * 1024;
constexpr unsigned SPIN_CAP = 1u << 17;

DI f32x4 mfma(bf16x8 a, bf16x8 b, f32x4 c) { return __builtin_amdgcn_mfma_f32_16x16x32_bf16(a, b, c, 0, 0, 0); }
DI bf16x8 asb(u32x4 v) { return __builtin_bit_cast(bf16x8, v); }
DI rsrc_t mk_rsrc(const void* p, unsigned bytes) { return __builtin_amdgcn_make_buffer_rsrc((void*)p, 0, bytes, 0x00020000); }
template <unsigned LIMM, unsigned SIMM, bool SC1> DI void dma(rsrc_t rs, unsigned m0b, unsigned voff, unsigned sb) {
  unsigned t;
  if constexpr (SC1)
    asm volatile("s_add_u32 m0, %1, %5\n\ts_add_u32 %0, %4, %6\n\tbuffer_load_dwordx4 %2, %3, %0 offen sc1 lds"
                 : "=&s"(t) : "s"(m0b), "v"(voff), "s"(rs), "s"(sb), "n"(LIMM), "n"(SIMM) : "m0", "scc");
  else
    asm volatile("s_add_u32 m0, %1, %5\n\ts_add_u32 %0, %4, %6\n\tbuffer_load_dwordx4 %2, %3, %0 offen lds"
                 : "=&s"(t) : "s"(m0b), "v"(voff), "s"(rs), "s"(sb), "n"(LIMM), "n"(SIMM) : "m0", "scc");
}
DI void ld128_sc1(u32x4& v, rsrc_t rs, unsigned voff, unsigned soff) {
  asm volatile("buffer_load_dwordx4 %0, %1, %2, %3 offen sc1" : "=v"(v) : "v"(voff), "s"(rs), "s"(soff));
}
DI void ld64(u32x2& v, rsrc_t rs, unsigned voff, unsigned soff) {
  asm volatile("buffer_load_dwordx2 %0, %1, %2, %3 offen" : "=v"(v) : "v"(voff), "s"(rs), "s"(soff));
}
DI void ld64_sc1(u32x2& v, rsrc_t rs, unsigned voff, unsigned soff) {
  asm volatile("buffer_load_dwordx2 %0, %1, %2, %3 offen sc1" : "=v"(v) : "v"(voff), "s"(rs), "s"(soff));
}
template <class T> DI void tie(T& v) { asm volatile("" : "+v"(v)); }
template <class T> DI void tie2(T& a, T& b) { asm volatile("" : "+v"(a), "+v"(b)); }
template <class T> DI void tie4(T& a, T& b, T& c, T& d) { asm volatile("" : "+v"(a), "+v"(b), "+v"(c), "+v"(d)); }
DI unsigned opaque(unsigned v) { asm("" : "+v"(v)); return v; }
DI void st128_sc1(u32x4 v, rsrc_t rs, unsigned voff, unsigned soff) {
  asm volatile("buffer_store_dwordx4 %0, %1, %2, %3 offen sc1\n\ts_nop 1" ::"v"(v), "v"(voff), "s"(rs), "s"(soff));
}
DI void st64_sc1(u32x2 v, rsrc_t rs, unsigned voff, unsigned soff) {
  asm volatile("buffer_store_dwordx2 %0, %1, %2, %3 offen sc1" ::"v"(v), "v"(voff), "s"(rs), "s"(soff));
}
DI void st64_sc1_w0(u32x2 v, rsrc_t rs, unsigned voff, unsigned soff, int w) {
  asm volatile("s_cmp_lg_u32 %4, 0\n\ts_cbranch_scc1 .Ls%=\n\tbuffer_store_dwordx2 %0, %1, %2, %3 offen sc1\n.Ls%=:"
               ::"v"(v), "v"(voff), "s"(rs), "s"(soff), "s"(w) : "scc");
}
DI bf16x8 lds_frag(unsigned a) { return *(LAS const bf16x8*)(size_t)a; }
DI bf16x8 lds_tr(unsigned a0, unsigned a1) {
  bf16x4 lo = __builtin_amdgcn_ds_read_tr16_b64_v4bf16((LAS bf16x4*)(size_t)a0);
  bf16x4 hi = __builtin_amdgcn_ds_read_tr16_b64_v4bf16((LAS bf16x4*)(size_t)a1);
  return __builtin_shufflevector(lo, hi, 0, 1, 2, 3, 4, 5, 6, 7);
}
DI unsigned pk2v(f32x2 v) { return __builtin_bit_cast(unsigned, __builtin_convertvector(v, bf16x2)); }
DI unsigned pk2(float a, float b) { return pk2v(f32x2{a, b}); }
DI float bf_lo(unsigned u) { return __builtin_bit_cast(float, u << 16); }
DI float bf_hi(unsigned u) { return __builtin_bit_cast(float, u & 0xffff0000u); }
DI float rbf(float a) { return bf_lo(pk2(a, a)); }
DI f32x2 fma2(f32x2 a, f32x2 b, f32x2 c) { return __builtin_elementwise_fma(a, b, c); }
DI f32x2 bf2(unsigned u) { return f32x2{bf_lo(u), bf_hi(u)}; }
DI float imax(float a, int lo) { return __builtin_bit_cast(float, __builtin_elementwise_max(__builtin_bit_cast(int, a), lo)); }
DI float phi1(float a, float r, float rl, float ub, float eps, int lo) {
  return imax(fmaf(a, r, eps), lo) + __builtin_amdgcn_fmed3f(__builtin_amdgcn_exp2f(fmaf(a, rl, ub)), 0.f, 1.f);
}
DI void wave_sum2(float& a, float& b) {
#define DPPSTEP(ctl) "v_add_f32_dpp %0, %0, %0 " ctl "\n\tv_add_f32_dpp %1, %1, %1 " ctl "\n\ts_nop 0\n\t"
  asm volatile("s_nop 1\n\t" DPPSTEP("row_shr:1 row_mask:0xf bank_mask:0xf bound_ctrl:0") DPPSTEP("row_shr:2 row_mask:0xf bank_mask:0xf bound_ctrl:0")
               DPPSTEP("row_shr:4 row_mask:0xf bank_mask:0xf bound_ctrl:0") DPPSTEP("row_shr:8 row_mask:0xf bank_mask:0xf bound_ctrl:0")
               DPPSTEP("row_bcast:15 row_mask:0xa bank_mask:0xf") DPPSTEP("row_bcast:31 row_mask:0xc bank_mask:0xf")
               : "+v"(a), "+v"(b));
}
struct Base3 {
  unsigned b[3];
  DI void set(unsigned a) { b[0] = opaque(a); b[1] = opaque(a + 65536u); b[2] = opaque(a + 131072u); }
  template <int SLOT> DI unsigned at() const { return b[SLOT >> 3] + (unsigned)(SLOT & 7) * 8192u; }
};

__host__ __device__ __forceinline__ int map_c(int b) { return 4 * ((b & 7) >> 1) + (b >> 6); }
__host__ __device__ __forceinline__ int map_role(int b) {
  const int j = (b >> 3) & 7;
  return j + (b & 1) * 7 + (j == 7 ? 7 - 6 * (b & 1) : 0);
}

struct Params {
  const char* x; const char* wqkv; const char* wo; const char* w1; const char* b1; const char* w2; const char* b2;
  char* out; char* ws;
};

template <int N, int SL = 2> DI void flag_chk64(u32x2& v, rsrc_t ws, unsigned voff, u64 tag) {
  unsigned cnt;
  asm volatile("s_waitcnt vmcnt(%5)\n\ts_mov_b32 %1, %6\n"
               ".Lp%=:\n\tv_cmp_ne_u64 vcc, %4, %0\n\ts_cbranch_vccz .Ld%=\n\ts_sleep %7\n\t"
               "buffer_load_dwordx2 %0, %2, %3, 0 offen sc1\n\ts_waitcnt vmcnt(0)\n\ts_sub_u32 %1, %1, 1\n\ts_cbranch_scc0 .Lp%=\n"
               ".Ld%=:"
               : "+v"(v), "=&s"(cnt) : "v"(voff), "s"(ws), "s"(tag), "n"(N), "n"(SPIN_CAP), "n"(SL) : "vcc", "scc", "memory");
}
template <int N> DI void gran_chk(u32x2& v, rsrc_t ws, unsigned voff, u64 tag32) {
  unsigned cnt;
  u32x2 t;
  asm volatile("s_waitcnt vmcnt(%6)\n\ts_mov_b32 %1, %7\n"
               ".Lp%=:\n\tv_lshrrev_b64 %2, 32, %0\n\tv_cmp_ne_u64 vcc, %5, %2\n\ts_cbranch_vccz .Ld%=\n\ts_sleep 2\n\t"
               "buffer_load_dwordx2 %0, %3, %4, 0 offen sc1\n\ts_waitcnt vmcnt(0)\n\ts_sub_u32 %1, %1, 1\n\ts_cbranch_scc0 .Lp%=\n"
               ".Ld%=:"
               : "+v"(v), "=&s"(cnt), "=&v"(t) : "v"(voff), "s"(ws), "s"(tag32), "n"(N), "n"(SPIN_CAP) : "vcc", "scc", "memory");
}

#define I_W0(sl, s) dma<(sl) * 8192, (s) * 64 * QROW, false>(rq, m0w, vq, col0)
#define I_X(sl, s, h) dma<(sl) * 8192, (h) * 64 * XROW + (s) * 128, false>(rx, m0w, vx, xsrc)

template <bool HELPER>
DI void run(const Params& P, const int c, const int role, const int w, const rsrc_t rx, const rsrc_t rq, const unsigned vx, const unsigned vq) {
  int tid = threadIdx.x;
  tie(tid);
  const int lane = tid & 63;
  const int p = w & 3, hf = w >> 2;
  const int l15 = lane & 15, g = lane >> 4;
  const rsrc_t rws = mk_rsrc(P.ws, WS_BYTES);
  const rsrc_t rB = mk_rsrc(P.w1, D_ * W1ROW);
  const rsrc_t rC = HELPER ? mk_rsrc(P.wo, D_ * W2ROW) : mk_rsrc(P.w2, F_ * W2ROW);
  const unsigned m0w = (unsigned)w * 1024u;
  const unsigned vl = (unsigned)lane * 16u;
  const f32x4 zero4 = {0.f, 0.f, 0.f, 0.f};

  const int r8 = lane >> 3, ch = lane & 7;
  const int drow = 8 * w + r8;
  const unsigned vB = opaque(vq + (unsigned)(drow * (W1ROW - QROW)));
  const unsigned vC = opaque((unsigned)((HELPER ? r8 : drow) * W2ROW + ((ch ^ (((r8 >> 1) & 3) << 1)) << 4)));
  const int hs = role - 14;
  const unsigned xsrc = (unsigned)(c * 128 * XROW), col0 = (unsigned)(896 + role * 128), col1 = (unsigned)(role * 256 - 128);
  const int th = role >= 7, ct = role - 7 * th;
  const unsigned tok0 = (unsigned)(8 * c + 4 * th);
  const int hd = (c & 12) + 2 * hs + ((c >> 1) & 1), dt = c & 1;
  const int odd = hd & 1, win0 = (28 * hd) & ~7;

#define SB() __builtin_amdgcn_sched_barrier(0)
#define WAITBAR(n) asm volatile("s_waitcnt vmcnt(" #n ") lgkmcnt(0)\n\ts_barrier" ::: "memory")
#define BAR() asm volatile("s_waitcnt lgkmcnt(0)\n\ts_barrier" ::: "memory")
#define SETPRIO(n) __builtin_amdgcn_s_setprio(n)
#define WAIT(n) asm volatile("s_waitcnt vmcnt(" #n ")" ::: "memory")
#define I_WB(sl, ps, s) dma<(sl) * 8192, (ps) * 128 + (s) * 64 * W1ROW, false>(rB, m0w, vB, col1)
#define I_WQ(sl, s) dma<(sl) * 8192, (s) * 64 * QROW, false>(rq, m0w, vq, vsrc)
#define I_V(sl, v) dma<(sl) * 8192, ((v) < 7 ? 128 * (v) : 128 * ((v) - 7) + 64) * W2ROW, false>(rC, m0w, vC, v2src)
#define I_H(sl, u) dma<(sl) * 8192, (4 * ((u) % 7) + 2 * ((u) / 14)) * 1024, true>(rws, m0w, vl, (((u) / 7) & 1) ? hsrc : hsrc ^ hxor)
#define I_PQ(sl, i) dma<(sl) * 8192, (7 * (i) - ((i) & 1)) * 128, true>(rws, m0w, vl, pqsrc)
#define I_M(sl, i) dma<(sl) * 8192, (i) * 28 * 1024, true>(rws, m0w, vl, msrc)
#define I_Z(sl) dma<(sl) * 8192, WS_Z, true>(rws, m0w, lane < 8 ? vl : OOB, (unsigned)w * 128u)
#define I_XR(sl) dma<(sl) * 8192, 0, false>(rx, m0w, vC, (unsigned)(tok0 * 16u * XROW + ct * 128))
#define I_WO(sl, i) dma<(sl) * 8192, (i) * 256, false>(rC, m0w, vC, wosrc)
#define I_KV(sl, i, k) dma<(sl) * 8192, ((k) == 0 ? WS_K : WS_V) + ((i) & 3) * KVT, true>(rws, m0w, vkv, ((k) == 0 ? k0src : (k) == 1 ? v0src : v1src) + ((i) < 4 ? kvlo : 4 * KVT - kvlo))
  const unsigned vsrc = (unsigned)(ct * 128);
  const unsigned v2src = vsrc + (unsigned)(th * 896 * W2ROW);
  const unsigned hsrc = WS_H + ((tok0 + (unsigned)(w >> 1)) * 56u + (unsigned)(28 * th + (w & 1))) * 1024u;
  const unsigned hxor = hsrc ^ (WS_H + (((tok0 ^ 4u) + (unsigned)(w >> 1)) * 56u + (unsigned)(28 * th + (w & 1))) * 1024u);
  const unsigned pqsrc = WS_PQ + (tok0 + (unsigned)p) * 14336u + (unsigned)hf * 7168u;
  const unsigned msrc = WS_M + (unsigned)((8 * hf * 28 + 4 * ct + p) * 1024);
  const unsigned wosrc = (unsigned)((win0 + 8 * p) * W2ROW + hf * 128);
  const unsigned ksrc = (unsigned)(8 * w * (int)KVT + win0 * 16), k0src = ksrc + (unsigned)(dt * 256), kvlo = (unsigned)dt * 4u * KVT;
  const unsigned v0src = ksrc + (unsigned)(odd * 256), v1src = ksrc + (unsigned)((1 - odd) * 256);
  const unsigned vkv = opaque((unsigned)(g * (int)KVG + l15 * 16));

  u64 tag;
#define ROW_STAT(n, sl) do { \
    asm volatile("s_waitcnt vmcnt(" #n ")" ::: "memory"); \
    const u32x2 ep_ = *(LAS const u32x2*)(size_t)(16 * 8192u + m0w); \
    tag = (((u64)(unsigned)__builtin_amdgcn_readfirstlane(ep_.y) << 32) | (unsigned)__builtin_amdgcn_readfirstlane(ep_.x)) * 0x9E3779B97F4A7C15ull + 0x632BE59BD9B4E019ull; \
    const u32x4 row = *(LAS const u32x4*)(size_t)((sl) * 8192u + m0w + vl); \
    const f32x2 a_ = bf2(row.x), b_ = bf2(row.y), c_ = bf2(row.z), d_ = bf2(row.w); \
    const f32x2 s_ = (a_ + b_) + (c_ + d_), q_ = fma2(a_, a_, b_ * b_) + fma2(c_, c_, d_ * d_); \
    float sm_ = s_.x + s_.y, sq_ = q_.x + q_.y; \
    wave_sum2(sm_, sq_); \
    const float mean_ = sm_ * (1.f / D_); \
    const float var_ = rbf(imax(sq_ * (1.f / D_) - mean_ * mean_, 0)); \
    const u32x2 gr_ = {pk2(__builtin_amdgcn_sqrtf(rbf(var_ + 1e-5f)), mean_), (unsigned)tag}; \
    st64_sc1(gr_, rws, lane == 63 ? 0u : OOB, WS_ST + (unsigned)(128 * c + 8 * role + w) * 8u); \
  } while (0)

  bf16x8 xf[2][14], wb[2][4];
  f32x4 acc[2][2][2];
  Base3 ax[4], aw[4], awh[4];
  const unsigned v_pub = opaque(tid == 0 ? 0u : OOB);
#define SETUP_A() do { \
    const unsigned ax0_ = (unsigned)(((2 * p + hf) * 16 + l15) * 128 + ((g ^ ((l15 >> 1) & 7)) << 4)); \
    const int q_ = l15 >> 2, swz_ = ((q_ >> 1) & 1) | ((g & 1) << 1); \
    const unsigned aw0_ = (unsigned)((8 * g + q_) * 128 + (((2 * hf) ^ swz_) << 5) + ((lane & 3) << 3)); \
    _Pragma("unroll") for (int j = 0; j < 2; ++j) { \
      ax[j].set(ax0_ ^ (unsigned)(j * 2048)); ax[2 + j].set(ax0_ ^ (unsigned)(j * 2048 + 64)); \
      aw[j].set(aw0_ ^ (unsigned)(j * 32)); \
      awh[j].set(aw0_ ^ (unsigned)(j * 32) ^ (unsigned)((hf ^ th) << 6)); \
    } \
  } while (0)
#define RDW(b, j, half, sl) wb[b][j] = lds_tr(((half) ? awh : aw)[(j) & 1].template at<sl>() + ((j) >> 1) * 4096u, ((half) ? awh : aw)[(j) & 1].template at<sl>() + ((j) >> 1) * 4096u + 512u)
#define RDX(s, j, sl) xf[(j) & 1][2 * (s) + ((j) >> 1)] = lds_frag(ax[j].template at<sl>())
#define MM(set, s, b, j, kind) do { \
    constexpr bool z_ = (s) == 0 && (j) < 2; \
    _Pragma("unroll") for (int tt = 0; tt < ((kind) == 2 ? 1 : 2); ++tt) \
      acc[set][tt][(j) & 1] = (kind) == 1 ? mfma(xf[tt][2 * (s) + ((j) >> 1)], wb[b][j], z_ ? zero4 : acc[set][tt][(j) & 1]) \
                                          : mfma(wb[b][j], xf[tt][2 * (s) + ((j) >> 1)], z_ ? zero4 : acc[set][tt][(j) & 1]); \
    if ((kind) == 2) tie(acc[set][0][(j) & 1]); else tie2(acc[set][0][(j) & 1], acc[set][1][(j) & 1]); \
  } while (0)
#define MIX(part, i, nm, nv) do { tie(eo2[part][i]); _Pragma("unroll") for (int i_ = 0; i_ < (nm); ++i_) { \
    __builtin_amdgcn_sched_group_barrier(0x8, 1, 0); __builtin_amdgcn_sched_group_barrier(0x402, nv, 0); } } while (0)
  u32x2 flg;
  u32x4 sf[2];
  const unsigned v_st = opaque(WS_ST + (unsigned)(128 * c + 32 * p + (lane & 31)) * 8u), a_st = opaque(L_STAT + (unsigned)((32 * p + (lane & 31)) * 4));
  const unsigned m_on = opaque((g & 1) == hf ? 0xffffffffu : 0u), m_lo = opaque(lane < 32 ? 0xffffffffu : 0u);
#define STAT_LD() ld64_sc1(flg, rws, v_st, 0)
#define STAT_PUT(n) do { \
    gran_chk<n>(flg, rws, v_st, tag & 0xffffffffull); \
    *(LAS float*)(size_t)a_st = __builtin_amdgcn_rcpf(bf_lo(flg.x)); \
    const unsigned nm_ = __builtin_amdgcn_perm(flg.x, flg.x, 0x03020302u) ^ 0x80008000u, sg_ = flg.x & 0xffffu; \
    sf[0] = u32x4{nm_ & m_on, sg_ & m_on, 0u, 0u}; sf[1] = u32x4{nm_ & ~m_on, sg_ & ~m_on, 0u, 0u}; \
    tie2(sf[0], sf[1]); \
  } while (0)
#define PUBLISH(off) st64_sc1_w0(__builtin_bit_cast(u32x2, tag), rws, v_pub, (off), w)
#define PUB_KV() PUBLISH(WS_FKV + (unsigned)(role * 16 + c) * 8u)
#define PUB_H(k) PUBLISH(WS_FH + (unsigned)(c * 64 + 16 * (k) + role) * 8u)
#define PUB_M() st64_sc1(__builtin_bit_cast(u32x2, tag), rws, tid < 16 ? (unsigned)tid * 256u : OOB, WS_FM + (unsigned)(2 * c + hs) * 8u)
#define PUB_EPOCH() do { \
    if (c == 0 && hs == 0 && w == 0) { \
      const unsigned o_ = WS_FM + (unsigned)(lane & 31) * 8u; \
      ld64_sc1(flg, rws, o_, 0); flag_chk64<0>(flg, rws, o_, tag); \
      PUBLISH(WS_EPOCH); \
    } \
  } while (0)
  auto flag_off = [&](int k) AI -> unsigned {
    if constexpr (HELPER) {
      const int lo = (28 * hd) >> 6, hi = (28 * hd + 27) >> 6;
      return WS_FKV + (unsigned)((((g & 1) ? hi : lo) + ((g >> 1) ? 7 : 0)) * 16 + l15) * 8u;
    } else {
      if (k < 2) return WS_FH + (unsigned)(c * 64 + 16 * k + 7 * th + (l15 < 7 ? l15 : 0)) * 8u;
      if (k == 3) return WS_FH + (unsigned)(c * 64 + 48 + ct + 7 * (1 - th)) * 8u;
      return lane < 32 ? WS_FM + (unsigned)(c * 32 + lane) * 8u : WS_FH + (unsigned)(c * 64 + 32 + (l15 < 14 ? l15 : 0)) * 8u;
    }
  };
#define FLAG_LD(k) ld64_sc1(flg, rws, flag_off(k), 0)
#define FLAG_CHK(k, n) flag_chk64<n>(flg, rws, flag_off(k), tag)

  u32x2 cg;
  unsigned bias;
  f32x4 rr[2], rl[2];
  float rq0, rq1;
  auto cs_off = [&](int ps) AI -> unsigned {
    const int line = ps == 0 ? 7 + role : ps == 3 ? ct : 21 + 2 * role + (ps - 1);
    return WS_CS + (unsigned)((line * 64 + 32 * (ps == 3 ? th : hf) + (lane >> 5) * 16 + l15) * 8);
  };
#define CS_LD(ps) do { \
    if ((ps) == 1 || (ps) == 2) asm volatile("buffer_load_ushort %0, %1, %2, %3 offen" : "=v"(bias) \
      : "v"((unsigned)(((lane >> 5) * 16 + l15) * 2)), "s"(mk_rsrc(P.b1, F_ * 2)), "s"((unsigned)((128 * role + 64 * ((ps) - 1) + 32 * hf) * 2))); \
    ld64_sc1(cg, rws, cs_off(ps), 0); \
  } while (0)
#define EPI_LD() do { \
    _Pragma("unroll") for (int tt = 0; tt < 2; ++tt) rr[tt] = *(LAS const f32x4*)(size_t)(L_STAT + (unsigned)(((2 * p + (tt ^ hf)) * 16 + 4 * g) * 4)); \
  } while (0)
  u32x4 cf[2];
  auto cs_chk = [&](auto psc, auto nc) AI {
    constexpr int ps = decltype(psc)::value;
    gran_chk<decltype(nc)::value>(cg, rws, cs_off(ps), tag & 0xffffffffull);
    if constexpr (ps == 1 || ps == 2) tie(bias); else bias = 0u;
    if constexpr (ps == 3) rq0 = *(LAS const float*)(size_t)(L_STAT + (unsigned)(((2 * p + hf) * 16 + l15) * 4));
    cf[0] = u32x4{cg.x & m_lo, bias & m_lo, 0u, 0u}; cf[1] = u32x4{cg.x & ~m_lo, bias & ~m_lo, 0u, 0u};
    tie2(cf[0], cf[1]);
  };
  auto epi_rk = [&](auto psc, auto kc) AI {
    constexpr int ps = decltype(psc)::value, kind = decltype(kc)::value, set = ps & 1;
#pragma unroll
    for (int tt = 0; tt < (kind == 2 ? 1 : 2); ++tt)
#pragma unroll
      for (int nt = 0; nt < 2; ++nt)
        acc[set][tt][nt] = kind == 1 ? mfma(asb(sf[tt]), asb(cf[nt]), acc[set][tt][nt]) : mfma(asb(cf[nt]), asb(sf[tt]), acc[set][tt][nt]);
    tie2(acc[set][0][0], acc[set][0][1]);
    if constexpr (kind != 2) tie2(acc[set][1][0], acc[set][1][1]);
    if constexpr (ps == 0) { rl[0] = rr[0] * 1.44269504088896341f; rl[1] = rr[1] * 1.44269504088896341f; }
    if constexpr (ps == 3) rq1 = rq0 * 1.44269504088896341f;
  };
  unsigned eo2[2][4];
  const bool isphi = role < 7;
  const int phi_lo = isphi ? 0x358637bd : (int)0x80000000;
  const float phi_ub = isphi ? 0.f : -1e30f, phi_eps = isphi ? 1e-6f : 0.f;
  auto epi_c = [&](auto psc, auto partc, auto ic) AI {
    constexpr int ps = decltype(psc)::value, part = decltype(partc)::value, i = decltype(ic)::value, h = i >> 1, e = 2 * (i & 1), set = ps & 1;
    unsigned (&eo)[4] = eo2[part];
    if constexpr (ps == 0) {
      const f32x4 a4 = acc[set][h][part];
      eo[i] = pk2(phi1(a4[e], rr[h][e], rl[h][e], phi_ub, phi_eps, phi_lo), phi1(a4[e + 1], rr[h][e + 1], rl[h][e + 1], phi_ub, phi_eps, phi_lo));
    } else {
      const f32x4 a4 = acc[set][part][h];
      if constexpr (ps == 3) eo[i] = pk2(phi1(a4[e], rq0, rq1, 0.f, 1e-6f, 0x358637bd), phi1(a4[e + 1], rq0, rq1, 0.f, 1e-6f, 0x358637bd));
      else { const float m0 = imax(a4[e], 0), m1 = imax(a4[e + 1], 0); eo[i] = pk2(m0 * m0, m1 * m1); }
    }
  };
  auto epi_st = [&](auto psc, auto partc) AI {
    constexpr int ps = decltype(psc)::value, part = decltype(partc)::value;
    unsigned (&eo)[4] = eo2[part];
    const unsigned tile = (unsigned)(8 * c + 2 * p + (part ^ hf));
    if constexpr (ps == 0) {
      st128_sc1(u32x4{hf ? eo[2] : eo[0], hf ? eo[3] : eo[1], hf ? eo[0] : eo[2], hf ? eo[1] : eo[3]}, rws, vkv,
                WS_K + (unsigned)(th * (int)(WS_V - WS_K) + (4 * c + p) * (int)KVT + (ct * 64 + 16 * (2 * hf + part)) * 16));
    } else if constexpr (ps == 3) {
      const unsigned dst = WS_PQ + (tile * 14u + (unsigned)(2 * ct + th)) * 1024u;
      st64_sc1(u32x2{eo[0], eo[1]}, rws, vl >> 1, dst); st64_sc1(u32x2{eo[2], eo[3]}, rws, vl >> 1, dst + 512u);
    } else {
      st128_sc1(u32x4{eo[0], eo[1], eo[2], eo[3]}, rws, vl, WS_H + (tile * 56u + (unsigned)(4 * role + 2 * (ps - 1) + hf)) * 1024u);
    }
  };
#define IC(n) std::integral_constant<int, (n)>{}
#define CS_CHK(ps, n) cs_chk(IC(ps), IC(n))
#define EPI_RK(ps, kind) epi_rk(IC(ps), IC(kind))
#define EPI_C(ps, part, i) epi_c(IC(ps), IC(part), IC(i))
#define EPI_ST(ps, part) epi_st(IC(ps), IC(part))

  if constexpr (!HELPER) {
    const int tp = w & 1, cp = (w >> 1) & 1, kh = w >> 2;
    bf16x8 wr[14][2], hb[3][2];
    u32x4 mf[2][2][2], pq[2][2];
    f32x4 fb[2][2][2], sa[2], md[2][2][2], gt[2];
    u32x4 pr[2];
    float inv[2][2], r2;
    Base3 bw[2], bh, bm, bM;
    u32x2 rb2[2], rxr[2];
    const unsigned ocol = (unsigned)((ct * 64 + 32 * cp + 4 * g) * 2);
    const unsigned orow = ((tok0 + (unsigned)(2 * tp + kh)) * 16u + (unsigned)l15) * XROW;
#define SETUP_B() do { \
      const int row_ = 32 * kh + 4 * g + (l15 >> 2); \
      const unsigned b0_ = (unsigned)(row_ * 128 + (((2 * cp) ^ ((row_ >> 1) & 3)) << 5) + ((lane & 3) << 3)); \
      bw[0].set(b0_); bw[1].set(b0_ ^ 32u); \
      bh.set((unsigned)((4 * tp + 3 * kh) * 1024) + vl);        \
      bm.set(((unsigned)((4 * tp + 3 * kh) * 1024) ^ 2048u) + vl); \
    } while (0)
#define RDV(i, sl) do { _Pragma("unroll") for (int t = 0; t < 2; ++t) wr[i][t] = lds_tr(bw[t].template at<sl>(), bw[t].template at<sl>() + 2048u); } while (0)
#define RDH(b, sl) do { hb[b][0] = lds_frag(bh.template at<sl>()); hb[b][1] = lds_frag(bm.template at<sl>()); } while (0)
#define MMB(b, i, o, z) do { \
      _Pragma("unroll") for (int t = 0; t < 2; ++t) _Pragma("unroll") for (int a = 0; a < 2; ++a) fb[o][a][t] = mfma(wr[i][t], hb[b][a], (z) ? zero4 : fb[o][a][t]); \
      tie4(fb[o][0][0], fb[o][0][1], fb[o][1][0], fb[o][1][1]); \
    } while (0)
#define P_ST() do { _Pragma("unroll") for (int t = 0; t < 2; ++t) \
      st128_sc1(__builtin_bit_cast(u32x4, fb[0][0][t] + gt[t]), rws, vl, WS_P + (unsigned)(((c * 14 + role) * 8 + w) * 2048 + t * 1024)); } while (0)
#define P_LD() do { _Pragma("unroll") for (int t = 0; t < 2; ++t) \
      ld128_sc1(pr[t], rws, vl, WS_P + (unsigned)(((c * 14 + ct + 7 * (1 - th)) * 8 + w) * 2048 + t * 1024)); } while (0)
#define LD_B2() do { const rsrc_t rbb_ = mk_rsrc(P.b2, D_ * 2); ld64(rb2[0], rbb_, ocol, 0); ld64(rb2[1], rbb_, ocol, 32); } while (0)
#define RDXR(sl) do { \
      const unsigned a_ = (sl) * 8192u + (unsigned)(((2 * tp + kh) * 16 + l15) * 128 + (((2 * cp) ^ ((l15 >> 1) & 3)) << 5) + g * 8); \
      rxr[0] = *(LAS const u32x2*)(size_t)a_; rxr[1] = *(LAS const u32x2*)(size_t)(a_ ^ 32u); \
    } while (0)
    f32x4 eps4;
    bf16x8 zf[2][2];
    unsigned bz;
    Base3 bs;
#define SETUP_S(zs) do { \
      eps4 = f32x4{1e-6f, 1e-6f, 1e-6f, 1e-6f}; tie(eps4); \
      bM.set((unsigned)(cp * 2048 + lane * 8)); \
      bs.set((unsigned)((2 * tp + kh) * 1024 + lane * 8)); \
      bz = opaque((zs) * 8192u + (unsigned)(g * 16)); \
    } while (0)
#define RD2(dst, a) do { const u32x2 lo_ = *(volatile LAS const u32x2*)(size_t)(a), hi_ = *(volatile LAS const u32x2*)(size_t)((a) + 512u); dst = u32x4{lo_.x, lo_.y, hi_.x, hi_.y}; } while (0)
#define RDS(h, sq, sl) do { \
      _Pragma("unroll") for (int a = 0; a < 2; ++a) { \
        zf[(h) & 1][a] = lds_frag(bz + a * 4096u + ((h) >> 1) * 1024u + ((h) & 1) * 64u); \
        RD2(pq[(h) & 1][a], bs.template at<sq>() + a * 4096u); \
        RD2(mf[(h) & 1][a][0], bM.template at<sl>() + a * 4096u); RD2(mf[(h) & 1][a][1], bM.template at<sl>() + a * 4096u + 1024u); \
      } \
    } while (0)
#define MMS(h) do { \
      _Pragma("unroll") for (int a = 0; a < 2; ++a) { \
        inv[(h) & 1][a] = mfma(zf[(h) & 1][a], asb(pq[(h) & 1][a]), eps4)[0]; \
        _Pragma("unroll") for (int t = 0; t < 2; ++t) md[(h) & 1][a][t] = mfma(asb(mf[(h) & 1][a][t]), asb(pq[(h) & 1][a]), zero4); \
      } \
      tie2(inv[(h) & 1][0], inv[(h) & 1][1]); tie4(md[(h) & 1][0][0], md[(h) & 1][0][1], md[(h) & 1][1][0], md[(h) & 1][1][1]); \
    } while (0)
#define FMS(h) do { \
      _Pragma("unroll") for (int a = 0; a < 2; ++a) { \
        const float f_ = __builtin_amdgcn_rcpf(inv[(h) & 1][a]); \
        _Pragma("unroll") for (int t = 0; t < 2; ++t) \
          sa[t] = ((h) == 0 && a == 0) ? md[(h) & 1][a][t] * f_ : __builtin_elementwise_fma(md[(h) & 1][a][t], f32x4{f_, f_, f_, f_}, sa[t]); \
      } \
      tie2(sa[0], sa[1]); \
    } while (0)
#define LD_R2() do { tie2(rb2[0], rb2[1]); r2 = *(LAS const float*)(size_t)(L_STAT + (unsigned)(((4 * th + 2 * tp + kh) * 16 + l15) * 4)); } while (0)
#define MIXS(nm, nv) do { _Pragma("unroll") for (int i_ = 0; i_ < (nm); ++i_) { \
      __builtin_amdgcn_sched_group_barrier(0x8, 1, 0); __builtin_amdgcn_sched_group_barrier(0x402, nv, 0); } } while (0)
#define XCH(v, s0, s1) do { *(LAS f32x4*)(size_t)((s0) * 8192u + m0w + vl) = v[1][0]; *(LAS f32x4*)(size_t)((s1) * 8192u + m0w + vl) = v[1][1]; } while (0)
#define XCH_LD(v, s0, s1) do { \
      v[0] = *(LAS const f32x4*)(size_t)((s0) * 8192u + (m0w ^ 4096u) + vl); v[1] = *(LAS const f32x4*)(size_t)((s1) * 8192u + (m0w ^ 4096u) + vl); \
    } while (0)
    auto gate = [&](int t) AI {
      const float q = r2 * r2;
      const f32x2 b0 = bf2(rb2[t].x), b1 = bf2(rb2[t].y);
      const f32x4 y = __builtin_elementwise_fma((fb[1][0][t] + gt[t]) + __builtin_bit_cast(f32x4, pr[t]), f32x4{q, q, q, q}, f32x4{b0.x, b0.y, b1.x, b1.y});
      const f32x4 u = y * -1.44269504088896341f;
#pragma unroll
      for (int r = 0; r < 4; ++r) gt[t][r] = y[r] * __builtin_amdgcn_rcpf(1.f + __builtin_amdgcn_exp2f(u[r]));
      tie(gt[t]);
    };
#define GATE(t) gate(t)
    auto fin = [&]() AI {
      const rsrc_t ro = mk_rsrc(P.out, S_ * XROW);
      u32x2 d[2];
#pragma unroll
      for (int t = 0; t < 2; ++t) {
        const f32x2 x0 = bf2(rxr[t].x), x1 = bf2(rxr[t].y);
        const f32x4 o = __builtin_elementwise_fma(sa[t], gt[t], f32x4{x0.x, x0.y, x1.x, x1.y});
        d[t] = u32x2{pk2(o[0], o[1]), pk2(o[2], o[3])};
      }
      const auto sx = __builtin_amdgcn_permlane16_swap(d[0].x, d[1].x, false, false), sy = __builtin_amdgcn_permlane16_swap(d[0].y, d[1].y, false, false);
      __builtin_amdgcn_raw_buffer_store_b128(u32x4{sx[0], sy[0], sx[1], sy[1]}, ro, orow + ocol + (unsigned)((g & 1) * 24), 0, 0);
    };
#define FINAL() fin()
  SETUP_A();
  I_W0(5, 1);
  I_X(3, 1, 0);
  I_X(4, 1, 1);
  I_W0(8, 2);
  I_X(6, 2, 0);
  I_X(7, 2, 1);
  ROW_STAT(9, 18);
  WAITBAR(7);
  RDW(0, 0, 0, 2);
  RDX(0, 0, 0); SB();
  I_W0(11, 3); SB();
  RDW(0, 1, 0, 2);
  RDX(0, 1, 0); SB();
  I_X(9, 3, 0); SB();
  RDW(0, 2, 0, 2);
  RDX(0, 2, 0); SB();
  I_X(10, 3, 1); SB();
  RDW(0, 3, 0, 2);
  RDX(0, 3, 0); SB();
  WAITBAR(7);
  SETPRIO(2);
  RDW(1, 0, 0, 5);
  RDX(1, 0, 3); SB();
  MM(0, 0, 0, 0, 1); SB();
  I_W0(2, 4); SB();
  RDW(1, 1, 0, 5);
  RDX(1, 1, 3); SB();
  MM(0, 0, 0, 1, 1); SB();
  SETPRIO(1);
  I_X(0, 4, 0); SB();
  RDW(1, 2, 0, 5);
  RDX(1, 2, 3); SB();
  MM(0, 0, 0, 2, 1); SB();
  SETPRIO(0);
  I_X(1, 4, 1); SB();
  RDW(1, 3, 0, 5);
  RDX(1, 3, 3); SB();
  MM(0, 0, 0, 3, 1); SB();
  WAITBAR(7);
  SETPRIO(2);
  RDW(0, 0, 0, 8);
  RDX(2, 0, 6); SB();
  MM(0, 1, 1, 0, 1); SB();
  I_W0(5, 5); SB();
  RDW(0, 1, 0, 8);
  RDX(2, 1, 6); SB();
  MM(0, 1, 1, 1, 1); SB();
  SETPRIO(1);
  I_X(3, 5, 0); SB();
  RDW(0, 2, 0, 8);
  RDX(2, 2, 6); SB();
  MM(0, 1, 1, 2, 1); SB();
  SETPRIO(0);
  I_X(4, 5, 1); SB();
  RDW(0, 3, 0, 8);
  RDX(2, 3, 6); SB();
  MM(0, 1, 1, 3, 1); SB();
  WAITBAR(6);
  SETPRIO(2);
  RDW(1, 0, 0, 11);
  RDX(3, 0, 9); SB();
  MM(0, 2, 0, 0, 1); SB();
  I_W0(8, 6); SB();
  RDW(1, 1, 0, 11);
  RDX(3, 1, 9); SB();
  MM(0, 2, 0, 1, 1); SB();
  SETPRIO(1);
  I_X(6, 6, 0); SB();
  RDW(1, 2, 0, 11);
  RDX(3, 2, 9); SB();
  MM(0, 2, 0, 2, 1); SB();
  SETPRIO(0);
  I_X(7, 6, 1); SB();
  RDW(1, 3, 0, 11);
  RDX(3, 3, 9); SB();
  MM(0, 2, 0, 3, 1); SB();
  WAITBAR(6);
  SETPRIO(2);
  RDW(0, 0, 0, 2);
  RDX(4, 0, 0); SB();
  MM(0, 3, 1, 0, 1); SB();
  I_WB(9, 1, 0); SB();
  RDW(0, 1, 0, 2);
  RDX(4, 1, 0); SB();
  MM(0, 3, 1, 1, 1); SB();
  SETPRIO(1);
  I_WB(10, 1, 1); SB();
  RDW(0, 2, 0, 2);
  RDX(4, 2, 0); SB();
  MM(0, 3, 1, 2, 1); SB();
  SETPRIO(0);
  I_WB(11, 1, 2); SB();
  RDW(0, 3, 0, 2);
  RDX(4, 3, 0); SB();
  MM(0, 3, 1, 3, 1); SB();
  WAITBAR(6);
  SETPRIO(2);
  STAT_LD();
  RDW(1, 0, 0, 5);
  RDX(5, 0, 3); SB();
  MM(0, 4, 0, 0, 1); SB();
  I_WB(0, 1, 3); SB();
  RDW(1, 1, 0, 5);
  RDX(5, 1, 3); SB();
  MM(0, 4, 0, 1, 1); SB();
  SETPRIO(1);
  I_WB(1, 1, 4); SB();
  RDW(1, 2, 0, 5);
  RDX(5, 2, 3); SB();
  MM(0, 4, 0, 2, 1); SB();
  SETPRIO(0);
  I_WB(2, 1, 5); SB();
  RDW(1, 3, 0, 5);
  RDX(5, 3, 3); SB();
  MM(0, 4, 0, 3, 1); SB();
  WAITBAR(7);
  SETPRIO(2);
  RDW(0, 0, 0, 8);
  RDX(6, 0, 6); SB();
  MM(0, 5, 1, 0, 1); SB();
  I_WB(3, 1, 6); SB();
  RDW(0, 1, 0, 8);
  RDX(6, 1, 6); SB();
  MM(0, 5, 1, 1, 1); SB();
  SETPRIO(1);
  I_WB(4, 2, 0); SB();
  RDW(0, 2, 0, 8);
  RDX(6, 2, 6); SB();
  MM(0, 5, 1, 2, 1); SB();
  SETPRIO(0);
  I_WB(5, 2, 1); SB();
  STAT_PUT(6); SB();
  RDW(0, 3, 0, 8);
  RDX(6, 3, 6); SB();
  MM(0, 5, 1, 3, 1); SB();
  WAITBAR(5);
  SETPRIO(2);
  EPI_LD();
  CS_LD(0);
  RDW(1, 0, 0, 9); SB();
  MM(0, 6, 0, 0, 1); SB();
  I_WB(6, 2, 2); SB();
  RDW(1, 1, 0, 9); SB();
  MM(0, 6, 0, 1, 1); SB();
  I_WB(7, 2, 3); SB();
  RDW(1, 2, 0, 9); SB();
  MM(0, 6, 0, 2, 1); SB();
  I_WB(8, 2, 4); SB();
  RDW(1, 3, 0, 9); SB();
  MM(0, 6, 0, 3, 1); SB();
  RDW(0, 0, 0, 10); SB();
  MM(1, 0, 1, 0, 0); SB();
  SETPRIO(1);
  I_WB(12, 2, 5); SB();
  CS_CHK(0, 4); SB();
  RDW(0, 1, 0, 10); SB();
  EPI_RK(0, 1);
  MM(1, 0, 1, 1, 0); SB();
  I_WB(13, 2, 6); SB();
  RDW(0, 2, 0, 10); SB();
  EPI_C(0, 0, 0);
  MM(1, 0, 1, 2, 0);
  MIX(0, 0, 2, 6); SB();
  I_V(14, 0); SB();
  RDW(0, 3, 0, 10); SB();
  EPI_C(0, 0, 1);
  MM(1, 0, 1, 3, 0);
  MIX(0, 1, 2, 6); SB();
  RDW(1, 0, 0, 11); SB();
  EPI_C(0, 0, 2);
  MM(1, 1, 0, 0, 0);
  MIX(0, 2, 2, 6); SB();
  I_V(15, 1); SB();
  RDW(1, 1, 0, 11); SB();
  EPI_C(0, 0, 3);
  MM(1, 1, 0, 1, 0);
  MIX(0, 3, 2, 6);
  EPI_ST(0, 0); SB();
  SETPRIO(0);
  I_V(16, 2); SB();
  RDW(1, 2, 0, 11); SB();
  EPI_C(0, 1, 0);
  MM(1, 1, 0, 2, 0);
  MIX(1, 0, 2, 6); SB();
  I_V(18, 3); SB();
  RDW(1, 3, 0, 11); SB();
  EPI_C(0, 1, 1);
  MM(1, 1, 0, 3, 0);
  MIX(1, 1, 2, 6); SB();
  RDW(0, 0, 0, 0); SB();
  EPI_C(0, 1, 2);
  MM(1, 2, 1, 0, 0);
  MIX(1, 2, 2, 6); SB();
  RDW(0, 1, 0, 0); SB();
  EPI_C(0, 1, 3);
  MM(1, 2, 1, 1, 0);
  MIX(1, 3, 2, 6);
  EPI_ST(0, 1); SB();
  RDW(0, 2, 0, 0); SB();
  MM(1, 2, 1, 2, 0); SB();
  RDW(0, 3, 0, 0); SB();
  MM(1, 2, 1, 3, 0); SB();
  BAR();
  SETPRIO(2);
  CS_LD(1);
  RDW(1, 0, 0, 1); SB();
  MM(1, 3, 0, 0, 0); SB();
  I_V(0, 4); SB();
  RDW(1, 1, 0, 1); SB();
  MM(1, 3, 0, 1, 0); SB();
  I_V(9, 5); SB();
  RDW(1, 2, 0, 1); SB();
  MM(1, 3, 0, 2, 0); SB();
  I_V(10, 6); SB();
  RDW(1, 3, 0, 1); SB();
  MM(1, 3, 0, 3, 0); SB();
  RDW(0, 0, 0, 2); SB();
  MM(1, 4, 1, 0, 0); SB();
  SETPRIO(1);
  I_WQ(11, 0); SB();
  RDW(0, 1, 0, 2); SB();
  MM(1, 4, 1, 1, 0); SB();
  I_WQ(17, 1); SB();
  RDW(0, 2, 0, 2); SB();
  MM(1, 4, 1, 2, 0); SB();
  RDW(0, 3, 0, 2); SB();
  MM(1, 4, 1, 3, 0); SB();
  LD_B2();
  RDW(1, 0, 0, 3); SB();
  MM(1, 5, 0, 0, 0); SB();
  SETPRIO(0);
  RDW(1, 1, 0, 3); SB();
  MM(1, 5, 0, 1, 0); SB();
  RDW(1, 2, 0, 3); SB();
  MM(1, 5, 0, 2, 0); SB();
  RDW(1, 3, 0, 3); SB();
  MM(1, 5, 0, 3, 0); SB();
  SETUP_B();
  WAITBAR(9);
  SETPRIO(2);
  PUB_KV();
  RDW(0, 0, 0, 4); SB();
  MM(1, 6, 1, 0, 0); SB();
  I_WQ(1, 2); SB();
  RDW(0, 1, 0, 4); SB();
  MM(1, 6, 1, 1, 0); SB();
  I_WQ(2, 3); SB();
  RDW(0, 2, 0, 4); SB();
  MM(1, 6, 1, 2, 0); SB();
  I_WQ(3, 4); SB();
  SETPRIO(1);
  CS_CHK(1, 10); SB();
  RDW(0, 3, 0, 4); SB();
  MM(1, 6, 1, 3, 0); SB();
  RDW(1, 0, 0, 5); SB();
  EPI_RK(1, 0);
  MM(0, 0, 0, 0, 0); SB();
  RDW(1, 1, 0, 5); SB();
  EPI_C(1, 0, 0);
  MM(0, 0, 0, 1, 0);
  MIX(0, 0, 2, 3); SB();
  RDW(1, 2, 0, 5); SB();
  EPI_C(1, 0, 1);
  MM(0, 0, 0, 2, 0);
  MIX(0, 1, 2, 3); SB();
  SETPRIO(0);
  RDW(1, 3, 0, 5); SB();
  EPI_C(1, 0, 2);
  MM(0, 0, 0, 3, 0);
  MIX(0, 2, 2, 3); SB();
  RDV(0, 14); SB();
  RDW(0, 0, 0, 6); SB();
  EPI_C(1, 0, 3);
  MM(0, 1, 1, 0, 0);
  MIX(0, 3, 2, 3);
  EPI_ST(1, 0); SB();
  RDW(0, 1, 0, 6); SB();
  EPI_C(1, 1, 0);
  MM(0, 1, 1, 1, 0);
  MIX(1, 0, 2, 3); SB();
  RDW(0, 2, 0, 6); SB();
  EPI_C(1, 1, 1);
  MM(0, 1, 1, 2, 0);
  MIX(1, 1, 2, 3); SB();
  RDW(0, 3, 0, 6); SB();
  EPI_C(1, 1, 2);
  MM(0, 1, 1, 3, 0);
  MIX(1, 2, 2, 3); SB();
  RDV(1, 15); SB();
  WAITBAR(9);
  SETPRIO(2);
  RDW(1, 0, 0, 7); SB();
  EPI_C(1, 1, 3);
  MM(0, 2, 0, 0, 0);
  MIX(1, 3, 2, 3);
  EPI_ST(1, 1); SB();
  I_WQ(4, 5); SB();
  RDW(1, 1, 0, 7); SB();
  MM(0, 2, 0, 1, 0); SB();
  I_WQ(5, 6); SB();
  RDW(1, 2, 0, 7); SB();
  MM(0, 2, 0, 2, 0); SB();
  I_V(6, 7); SB();
  RDW(1, 3, 0, 7); SB();
  MM(0, 2, 0, 3, 0); SB();
  RDV(2, 16); SB();
  CS_LD(2);
  RDW(0, 0, 0, 8); SB();
  MM(0, 3, 1, 0, 0); SB();
  SETPRIO(1);
  I_V(14, 8); SB();
  RDW(0, 1, 0, 8); SB();
  MM(0, 3, 1, 1, 0); SB();
  I_V(15, 9); SB();
  RDW(0, 2, 0, 8); SB();
  MM(0, 3, 1, 2, 0); SB();
  RDW(0, 3, 0, 8); SB();
  MM(0, 3, 1, 3, 0); SB();
  RDV(3, 18); SB();
  RDW(1, 0, 0, 12); SB();
  MM(0, 4, 0, 0, 0); SB();
  SETPRIO(0);
  RDW(1, 1, 0, 12); SB();
  MM(0, 4, 0, 1, 0); SB();
  RDW(1, 2, 0, 12); SB();
  MM(0, 4, 0, 2, 0); SB();
  RDW(1, 3, 0, 12); SB();
  MM(0, 4, 0, 3, 0); SB();
  RDV(4, 0); SB();
  WAITBAR(7);
  SETPRIO(2);
  PUB_H(0);
  RDW(0, 0, 0, 13); SB();
  MM(0, 5, 1, 0, 0); SB();
  I_V(0, 10); SB();
  RDW(0, 1, 0, 13); SB();
  MM(0, 5, 1, 1, 0); SB();
  I_V(7, 11); SB();
  RDW(0, 2, 0, 13); SB();
  MM(0, 5, 1, 2, 0); SB();
  I_V(8, 12); SB();
  SETPRIO(1);
  RDW(0, 3, 0, 13); SB();
  MM(0, 5, 1, 3, 0); SB();
  RDV(5, 9); SB();
  RDW(1, 0, 1, 11); SB();
  MM(0, 6, 0, 0, 0); SB();
  I_V(12, 13); SB();
  RDW(1, 1, 1, 11); SB();
  MM(0, 6, 0, 1, 0); SB();
  SETPRIO(0);
  I_XR(16); SB();
  RDW(1, 2, 1, 11); SB();
  MM(0, 6, 0, 2, 0); SB();
  CS_CHK(2, 7); SB();
  RDW(1, 3, 1, 11); SB();
  MM(0, 6, 0, 3, 0); SB();
  RDV(6, 10); SB();
  WAITBAR(5);
  SETPRIO(2);
  RDW(0, 0, 1, 17); SB();
  EPI_RK(2, 0);
  MM(1, 0, 1, 0, 2); SB();
  RDW(0, 1, 1, 17); SB();
  EPI_C(2, 0, 0);
  MM(1, 0, 1, 1, 2);
  MIX(0, 0, 1, 6); SB();
  RDW(0, 2, 1, 17); SB();
  EPI_C(2, 0, 1);
  MM(1, 0, 1, 2, 2);
  MIX(0, 1, 1, 6); SB();
  SETPRIO(1);
  RDW(0, 3, 1, 17); SB();
  EPI_C(2, 0, 2);
  MM(1, 0, 1, 3, 2);
  MIX(0, 2, 1, 6); SB();
  RDV(7, 6); SB();
  FLAG_LD(0);
  RDW(1, 0, 1, 1); SB();
  EPI_C(2, 0, 3);
  MM(1, 1, 0, 0, 2);
  MIX(0, 3, 1, 6);
  EPI_ST(2, 0); SB();
  RDW(1, 1, 1, 1); SB();
  EPI_C(2, 1, 0);
  MM(1, 1, 0, 1, 2);
  MIX(1, 0, 1, 6); SB();
  SETPRIO(0);
  RDW(1, 2, 1, 1); SB();
  EPI_C(2, 1, 1);
  MM(1, 1, 0, 2, 2);
  MIX(1, 1, 1, 6); SB();
  RDW(1, 3, 1, 1); SB();
  EPI_C(2, 1, 2);
  MM(1, 1, 0, 3, 2);
  MIX(1, 2, 1, 6); SB();
  RDV(8, 14); SB();
  WAITBAR(5);
  SETPRIO(2);
  RDW(0, 0, 1, 2); SB();
  EPI_C(2, 1, 3);
  MM(1, 2, 1, 0, 2);
  MIX(1, 3, 1, 6);
  EPI_ST(2, 1); SB();
  RDW(0, 1, 1, 2); SB();
  MM(1, 2, 1, 1, 2); SB();
  RDW(0, 2, 1, 2); SB();
  MM(1, 2, 1, 2, 2); SB();
  RDW(0, 3, 1, 2); SB();
  MM(1, 2, 1, 3, 2); SB();
  SETPRIO(1);
  RDV(9, 15); SB();
  CS_LD(3);
  RDW(1, 0, 1, 3); SB();
  FLAG_CHK(0, 3);
  MM(1, 3, 0, 0, 2); SB();
  I_H(1, 0); SB();
  RDW(1, 1, 1, 3); SB();
  MM(1, 3, 0, 1, 2); SB();
  I_H(6, 1); SB();
  SETPRIO(0);
  RDW(1, 2, 1, 3); SB();
  MM(1, 3, 0, 2, 2); SB();
  I_H(9, 2); SB();
  RDW(1, 3, 1, 3); SB();
  MM(1, 3, 0, 3, 2); SB();
  RDV(10, 0); SB();
  BAR();
  SETPRIO(2);
  RDW(0, 0, 1, 4); SB();
  MM(1, 4, 1, 0, 2); SB();
  I_H(0, 3); SB();
  RDW(0, 1, 1, 4); SB();
  MM(1, 4, 1, 1, 2); SB();
  I_H(2, 4); SB();
  RDW(0, 2, 1, 4); SB();
  MM(1, 4, 1, 2, 2); SB();
  I_H(3, 5); SB();
  SETPRIO(1);
  RDW(0, 3, 1, 4); SB();
  MM(1, 4, 1, 3, 2); SB();
  RDV(11, 7); SB();
  RDXR(16);
  RDW(1, 0, 1, 5); SB();
  MM(1, 5, 0, 0, 2); SB();
  I_H(10, 6); SB();
  RDW(1, 1, 1, 5); SB();
  MM(1, 5, 0, 1, 2); SB();
  SETPRIO(0);
  I_H(11, 7); SB();
  RDW(1, 2, 1, 5); SB();
  MM(1, 5, 0, 2, 2); SB();
  I_H(13, 8); SB();
  RDW(1, 3, 1, 5); SB();
  MM(1, 5, 0, 3, 2); SB();
  RDV(12, 8); SB();
  WAITBAR(10);
  SETPRIO(2);
  PUB_H(1);
  MM(1, 6, 1, 0, 2); SB();
  I_H(4, 9); SB();
  MM(1, 6, 1, 1, 2); SB();
  SETPRIO(1);
  I_H(5, 10); SB();
  CS_CHK(3, 11); SB();
  MM(1, 6, 1, 2, 2); SB();
  I_H(7, 11); SB();
  SETPRIO(0);
  MM(1, 6, 1, 3, 2); SB();
  RDV(13, 12); SB();
  WAITBAR(9);
  SETPRIO(1);
  RDH(0, 1); SB();
  EPI_RK(3, 2); SB();
  EPI_C(3, 0, 0);
  MIX(0, 0, 1, 12); SB();
  EPI_C(3, 0, 1);
  MIX(0, 1, 1, 12); SB();
  I_H(8, 12);
  I_H(12, 13); SB();
  RDH(1, 6); SB();
  EPI_C(3, 0, 2);
  MIX(0, 2, 1, 12); SB();
  EPI_C(3, 0, 3);
  MIX(0, 3, 1, 12);
  EPI_ST(3, 0); SB();
  SB();
  RDH(2, 9); SB();
  MMB(0, 0, 0, 1); SB();
  SETPRIO(0);
  WAITBAR(9);
  SETPRIO(2);
  RDH(0, 0); SB();
  FLAG_LD(1);
  MMB(1, 1, 0, 0); SB();
  RDH(1, 2); SB();
  MMB(2, 2, 0, 0); SB();
  SETPRIO(1);
  RDH(2, 3); SB();
  MMB(0, 3, 0, 0); SB();
  SETPRIO(0);
  RDH(0, 10); SB();
  MMB(1, 4, 0, 0); SB();
  WAITBAR(0);
  SETPRIO(2);
  PUB_H(2);
  RDH(1, 11); SB();
  FLAG_CHK(1, 63);
  MMB(2, 5, 0, 0); SB();
  I_H(0, 14);
  I_H(1, 15); SB();
  RDH(2, 13); SB();
  MMB(0, 6, 0, 0); SB();
  SETPRIO(1);
  I_H(2, 16);
  I_H(3, 17); SB();
  RDH(0, 4); SB();
  MMB(1, 0, 1, 1); SB();
  SETPRIO(0);
  I_H(6, 18);
  I_H(9, 19); SB();
  RDH(1, 5); SB();
  MMB(2, 1, 1, 0); SB();
  I_H(10, 20);
  I_H(14, 21); SB();
  WAITBAR(4);
  SETPRIO(2);
  RDH(2, 0); SB();
  MMB(0, 2, 1, 0); SB();
  I_H(4, 22);
  I_H(5, 23); SB();
  RDH(0, 1); SB();
  MMB(1, 3, 1, 0); SB();
  SETPRIO(1);
  I_H(11, 24);
  I_H(13, 25); SB();
  RDH(1, 2); SB();
  MMB(2, 7, 0, 0); SB();
  SETPRIO(0);
  I_H(15, 26);
  I_H(16, 27); SB();
  RDH(2, 3); SB();
  MMB(0, 8, 0, 0); SB();
  WAITBAR(7);
  SETPRIO(2);
  RDH(0, 6); SB();
  FLAG_LD(2);
  MMB(1, 9, 0, 0); SB();
  RDH(1, 9); SB();
  MMB(2, 10, 0, 0); SB();
  SETPRIO(1);
  RDH(2, 10); SB();
  MMB(0, 11, 0, 0); SB();
  SETPRIO(0);
  RDH(0, 7); SB();
  FLAG_CHK(2, 0);
  MMB(1, 12, 0, 0); SB();
  I_Z(0);
  I_PQ(1, 0); SB();
  BAR();
  SETPRIO(2);
  RDH(1, 8); SB();
  MMB(2, 13, 0, 0); SB();
  XCH(fb[0], 2, 3); SB();
  SETPRIO(1);
  I_M(6, 0);
  I_PQ(7, 1); SB();
  RDH(2, 12); SB();
  MMB(0, 4, 1, 0); SB();
  I_M(9, 1);
  I_PQ(10, 2); SB();
  RDH(0, 14); SB();
  MMB(1, 5, 1, 0); SB();
  SETPRIO(0);
  I_M(17, 2);
  I_PQ(18, 3); SB();
  RDH(1, 4); SB();
  MMB(2, 6, 1, 0); SB();
  WAITBAR(5);
  SETPRIO(2);
  XCH_LD(gt, 2, 3); P_ST(); SB();
  RDH(2, 5); SB();
  SETUP_S(0);
  RDS(0, 1, 6); SB();
  MMB(0, 7, 1, 0); SB();
  I_M(4, 3);
  I_PQ(8, 4); SB();
  RDH(0, 11); SB();
  MMS(0);
  MMB(1, 8, 1, 0); SB();
  SETPRIO(1);
  I_M(12, 4);
  I_PQ(14, 5); SB();
  RDH(1, 13); SB();
  FMS(0);
  MMB(2, 9, 1, 0);
  MIXS(4, 4); SB();
  SETPRIO(0);
  WAITBAR(4);
  SETPRIO(2);
  PUB_H(3);
  RDH(2, 15); SB();
  RDS(1, 7, 9); SB();
  MMB(0, 10, 1, 0); SB();
  SETPRIO(1);
  I_M(1, 5);
  I_PQ(2, 6); SB();
  RDH(0, 16); SB();
  RDS(2, 10, 17); SB();
  MMS(1);
  MMB(1, 11, 1, 0); SB();
  SETPRIO(0);
  I_M(3, 6);
  I_PQ(5, 7); SB();
  WAITBAR(5);
  SETPRIO(2);
  RDS(3, 18, 4); SB();
  FMS(1);
  MMS(2);
  MMB(2, 12, 1, 0);
  MIXS(10, 2); SB();
  SETPRIO(1);
  I_M(9, 7); SB();
  FLAG_LD(3);
  RDS(4, 8, 12); SB();
  FMS(2);
  MMS(3);
  MMB(0, 13, 1, 0);
  MIXS(10, 2); SB();
  SETPRIO(0);
  XCH(fb[1], 6, 7); LD_R2(); SB();
  WAITBAR(1);
  SETPRIO(2);
  XCH_LD(gt, 6, 7); SB();
  RDS(5, 14, 1); SB();
  FMS(3);
  MMS(4);
  MIXS(6, 4); SB();
  RDS(6, 2, 3); SB();
  FMS(4);
  MMS(5);
  MIXS(6, 4); SB();
  SETPRIO(1);
  FLAG_CHK(3, 0);
  P_LD();
  RDS(7, 5, 9); SB();
  FMS(5);
  MMS(6);
  MIXS(6, 4); SB();
  FMS(6);
  MMS(7);
  MIXS(6, 4); SB();
  SETPRIO(0);
  asm volatile("s_waitcnt vmcnt(0)" : "+v"(pr[0]), "+v"(pr[1]));
  GATE(0); SB();
  FMS(7);
  MIXS(0, 4); SB();
  GATE(1); SB();
  FINAL();
  } else {
    const int hid = 2 * c + hs;
    int cl[2];
    cl[0] = hid < 14 ? 7 + hid : hid < 28 ? 21 + 2 * (hid - 14) : 22 + 2 * (hid - 28);
    cl[1] = hid < 10 ? 30 + 2 * hid : hid < 17 ? hid - 10 : -1;
    const bool cq[2] = {cl[0] < 21, cl[1] < 21};
    const rsrc_t rcs[2] = {cq[0] ? rq : rB, mk_rsrc(cq[1] ? P.wqkv : P.w1, cl[1] < 0 ? 0 : cq[1] ? D_ * QROW : D_ * W1ROW)};
    const unsigned vcs[2] = {cq[0] ? vq : vB, cq[1] ? vq : vB};
#define I_C(sl, r, s) dma<(sl) * 8192, 0, false>(rcs[r], m0w, vcs[r], (unsigned)((cq[r] ? cl[r] : cl[r] - 21) * 128 + (s) * 64 * (cq[r] ? QROW : W1ROW)))
#define I_PFW(sl, i) dma<(sl) * 8192, (i) * 8192, false>(mk_rsrc(P.w2, F_ * W2ROW), m0w, vl, (unsigned)(hid * 7 * 8192) + m0w)
    f32x4 cs;
    const unsigned a_c = opaque((unsigned)((8 * g + (l15 >> 2)) * 128 + ((p ^ (((l15 >> 3) & 1) | ((g & 1) << 1))) << 5) + ((lane & 3) << 3)));
    const bf16x8 ones = asb(u32x4{0x3f803f80u, 0x3f803f80u, 0x3f803f80u, 0x3f803f80u});
    bf16x8 cw[14];
#define CSUM(s, sl) do { \
      cw[2 * (s)] = lds_tr((sl) * 8192u + a_c, (sl) * 8192u + a_c + 512u); cw[2 * (s) + 1] = lds_tr((sl) * 8192u + a_c + 4096u, (sl) * 8192u + a_c + 4608u); \
      if ((s) == 6) { SB(); cs = zero4; _Pragma("unroll") for (int i = 0; i < 14; ++i) cs = mfma(ones, cw[i], cs); } \
    } while (0)
#define CS_ST(r) st64_sc1(u32x2{pk2(cs[0], cs[0] - rbf(cs[0])), (unsigned)tag}, rws, (tid < 256 && g == 0 && cl[r] >= 0) ? (unsigned)((16 * p + l15) * 8) : OOB, \
                          WS_CS + (unsigned)((cl[r] < 0 ? 0 : cl[r]) * 512))
    bf16x8 wof[4];
    f32x4 ka[2];
    const bool pad = odd ? l15 < 4 : l15 >= 12;
    Base3 bk;
    bk.set(m0w + vl);
    unsigned a_red, a_kvf, v_z, keep_z, keep_kv;
    const unsigned a_wo = opaque((unsigned)(hf * 4096 + (4 * g + (l15 >> 2)) * 128 + ((p ^ ((2 * g + (l15 >> 3)) & 3)) << 5) + ((lane & 3) << 3)));
#define SETUP_K(s0, s1) do { \
      const int ln_ = (tid >> 2) & 63, r_ = tid & 3, dd_ = 16 * dt + (ln_ & 15); \
      a_red = (hf ? (s1) : (s0)) * 8192u + (unsigned)(tid & 255) * 4u; \
      a_kvf = L_KVF + (unsigned)(ln_ * 16 + (hf ^ odd) * 8 + r_ * 2); \
      const bool dpad_ = odd ? dd_ < 4 : dd_ >= 28, epad_ = hf == 1 && (ln_ >> 4) == (odd ? 0 : 3); \
      v_z = epad_ && r_ == 0 ? (unsigned)(((dd_ >> 2) & 3) * 16 + (dd_ & 3) * 2) : OOB; \
      keep_z = dpad_ ? 0u : 0xffffu; keep_kv = epad_ ? 0u : keep_z; \
      tie4(a_red, a_kvf, v_z, keep_z); tie(keep_kv); \
    } while (0)
#define RDWO(i, sl) wof[i] = lds_tr((sl) * 8192u + a_wo, (sl) * 8192u + a_wo + 2048u)
    bf16x8 kf0, vf0;
    u32x4 vf1;
#define KVRD(s0, s1, s2) do { kf0 = lds_frag(bk.template at<s0>()); vf0 = lds_frag(bk.template at<s1>()); vf1 = __builtin_bit_cast(u32x4, lds_frag(bk.template at<s2>())); } while (0)
#define KVMM(i) do { \
      _Pragma("unroll") for (int e = 0; e < 4; ++e) vf1[e] = pad ? 0x3f803f80u : vf1[e]; \
      ka[0] = mfma(vf0, kf0, (i) == 0 ? zero4 : ka[0]); ka[1] = mfma(asb(vf1), kf0, (i) == 0 ? zero4 : ka[1]); \
      tie2(ka[0], ka[1]); \
    } while (0)
#define KV_PUT(s0, s1) do { *(LAS f32x4*)(size_t)bk.template at<s0>() = ka[0]; *(LAS f32x4*)(size_t)bk.template at<s1>() = ka[1]; } while (0)
    auto kv_red = [&]() AI {
      float t[8];
#pragma unroll
      for (int i = 0; i < 8; ++i) t[i] = *(LAS const float*)(size_t)(a_red + i * 1024u);
      const float s = ((t[0] + t[1]) + (t[2] + t[3])) + ((t[4] + t[5]) + (t[6] + t[7]));
      const unsigned v = pk2(s, s);
      *(LAS unsigned short*)(size_t)a_kvf = (unsigned short)(v & keep_kv);
      __builtin_amdgcn_raw_buffer_store_b16((unsigned short)(v & keep_z), rws, v_z, WS_Z + (unsigned)(hd * 64 + dt * 8), 16);
    };
#define KV_RED() kv_red()
    auto mk_m = [&]() AI {
      const bf16x8 kvf = lds_frag(L_KVF + vl);
#pragma unroll
      for (int i = 0; i < 4; ++i) {
        const f32x4 m = mfma(kvf, wof[i], zero4);
        st64_sc1(u32x2{pk2(m[0], m[1]), pk2(m[2], m[3])}, rws, (i == 3 && hf) ? OOB : (unsigned)lane * 8u,
                 WS_M + (unsigned)((hd * 28 + w + 8 * i) * 1024 + dt * 512));
      }
    };
#define MK_M() mk_m()
  I_C(3, 0, 0);
  I_C(4, 0, 1);
  I_C(5, 0, 2);
  I_C(6, 0, 3);
  I_C(7, 0, 4);
  I_C(8, 0, 5);
  I_C(9, 0, 6);
  ROW_STAT(10, 18);
  I_C(10, 1, 0);
  I_C(11, 1, 1);
  I_C(12, 1, 2);
  I_C(13, 1, 3);
  I_C(14, 1, 4);
  I_C(15, 1, 5);
  WAITBAR(7);
  CSUM(0, 3);
  CSUM(1, 4);
  CSUM(2, 5);
  CSUM(3, 6);
  CSUM(4, 7);
  CSUM(5, 8);
  CSUM(6, 9);
  CS_ST(0);
  I_C(0, 1, 6);
  I_PFW(17, 0);
  I_PFW(17, 1);
  I_PFW(17, 2);
  I_PFW(17, 3);
  I_PFW(17, 4);
  I_PFW(17, 5);
  I_PFW(17, 6);
  WAITBAR(7);
  CSUM(0, 10);
  CSUM(1, 11);
  CSUM(2, 12);
  CSUM(3, 13);
  CSUM(4, 14);
  CSUM(5, 15);
  CSUM(6, 0);
  CS_ST(1);
  I_WO(1, 0);
  I_WO(2, 1);
  I_WO(3, 2);
  I_WO(4, 3);
  WAITBAR(0);
  RDWO(0, 1);
  RDWO(1, 2);
  RDWO(2, 3);
  RDWO(3, 4);
  SETUP_K(0, 1);
  if (w == 0) { FLAG_LD(0); flag_chk64<0, 6>(flg, rws, flag_off(0), tag); }
  BAR();
  I_KV(0, 0, 0);
  I_KV(1, 0, 1);
  I_KV(2, 0, 2);
  I_KV(3, 1, 0);
  I_KV(4, 1, 1);
  I_KV(5, 1, 2);
  I_KV(6, 2, 0);
  I_KV(7, 2, 1);
  I_KV(8, 2, 2);
  I_KV(9, 3, 0);
  I_KV(10, 3, 1);
  I_KV(11, 3, 2);
  I_KV(12, 4, 0);
  I_KV(13, 4, 1);
  I_KV(14, 4, 2);
  I_KV(15, 5, 0);
  I_KV(16, 5, 1);
  I_KV(17, 5, 2);
  WAIT(15);
  KVRD(0, 1, 2); KVMM(0); SB();
  I_KV(0, 6, 0);
  I_KV(1, 6, 1);
  I_KV(2, 6, 2);
  WAIT(15);
  KVRD(3, 4, 5); KVMM(1); SB();
  I_KV(3, 7, 0);
  I_KV(4, 7, 1);
  I_KV(5, 7, 2);
  WAIT(15);
  KVRD(6, 7, 8); KVMM(2); SB();
  WAIT(12);
  KVRD(9, 10, 11); KVMM(3); SB();
  WAIT(9);
  KVRD(12, 13, 14); KVMM(4); SB();
  WAIT(6);
  KVRD(15, 16, 17); KVMM(5); SB();
  WAIT(3);
  KVRD(0, 1, 2); KVMM(6); SB();
  WAIT(0);
  KVRD(3, 4, 5); KVMM(7); SB();
  KV_PUT(0, 1);
  BAR();
  KV_RED();
  BAR();
  MK_M();
  WAITBAR(0);
  PUB_M();
  PUB_EPOCH();
  }
}

__attribute__((target("no-packed-fp32-ops"))) __global__ __launch_bounds__(512, 2) void fused_block_kernel(Params P) {
  __shared__ __attribute__((aligned(16))) char smem[L_BYTES];
  asm volatile("" ::"v"((unsigned)(size_t)(LAS char*)smem));
  const int b = blockIdx.x;
  const int c = map_c(b), role = map_role(b);
  const int lane = threadIdx.x & 63, w = __builtin_amdgcn_readfirstlane(threadIdx.x >> 6);
  {
    u64 pc;
    asm volatile("s_getpc_b64 %0" : "=s"(pc));
    dma<17 * 8192, 0, false>(mk_rsrc((const void*)(pc & ~255ull), CODE_BYTES), (unsigned)w * 1024u, (unsigned)lane * 16u, (unsigned)(((b >> 3) + 32 * w) * 1024));
  }
  const int ch = lane & 7, drow = 8 * w + (lane >> 3);
  unsigned vx = (unsigned)(drow * XROW + ((ch ^ ((drow >> 1) & 7)) << 4));
  unsigned vq = (unsigned)(drow * QROW + ((ch ^ ((((drow >> 1) & 1) | (((drow >> 3) & 1) << 1)) << 1)) << 4));
  unsigned xsrc = (unsigned)(c * 128 * XROW), col0 = (unsigned)(896 + role * 128), m0w = (unsigned)w * 1024u;
  unsigned vrow = lane < 56 ? (unsigned)lane * 16u : OOB, srow = (unsigned)((128 * c + 8 * role + w) * XROW);
  asm volatile("" : "+v"(vrow), "+s"(srow));
  unsigned nx = role >= 14 ? 0u : S_ * XROW, nq = role >= 14 ? 0u : D_ * QROW;
  asm volatile("" : "+v"(vx), "+v"(vq), "+s"(xsrc), "+s"(col0), "+s"(m0w), "+s"(nx), "+s"(nq));
  __builtin_amdgcn_sched_barrier(0);
  {
    dma<16 * 8192, WS_EPOCH, true>(mk_rsrc(P.ws, 16), m0w, vrow, 0u);
    dma<18 * 8192, 0, false>(mk_rsrc(P.x, S_ * XROW), m0w, vrow, srow);
    const rsrc_t rx = mk_rsrc(P.x, nx), rq = mk_rsrc(P.wqkv, nq);
  I_W0(2, 0);
  I_X(0, 0, 0);
  I_X(1, 0, 1);
  }
  __builtin_amdgcn_sched_barrier(0);
  const rsrc_t rx = mk_rsrc(P.x, S_ * XROW), rq = mk_rsrc(P.wqkv, D_ * QROW);
  if (role >= 14) run<true>(P, c, role, w, rx, rq, vx, vq);
  else run<false>(P, c, role, w, rx, rq, vx, vq);
}

extern "C" void kernel_launch(void* const* d_in, const int* in_sizes, int n_in, void* d_out, int out_size, void* d_ws, size_t ws_size,
                              hipStream_t stream) {
  Params P;
  P.x = (const char*)d_in[0]; P.wqkv = (const char*)d_in[1]; P.wo = (const char*)d_in[2];
  P.w1 = (const char*)d_in[4]; P.b1 = (const char*)d_in[5]; P.w2 = (const char*)d_in[6]; P.b2 = (const char*)d_in[7];
  P.out = (char*)d_out; P.ws = (char*)d_ws;
  fused_block_kernel<<<256, 512, 0, stream>>>(P);
}
```
